```python
import torch

def _softmax(x, axis=-1):
    return torch.softmax(x, dim=axis)
def _gelu(x):
    return torch.nn.functional.gelu(x, approximate="tanh")
def _silu(x):
    return torch.nn.functional.silu(x)
def _sigmoid(x):
    return torch.sigmoid(x)
def _layernorm(x, axis=-1, eps=1e-5):
    m = x.mean(dim=axis, keepdim=True)
    v = x.var(dim=axis, keepdim=True, unbiased=False)
    return (x - m) / torch.sqrt(v + eps)
def _scatter_add(tgt, idx, src, axis=0):
    return tgt.clone().index_add_(axis, idx, src)
def _segment_sum(src, seg, n):
    out = torch.zeros((n,) + src.shape[1:], dtype=src.dtype)
    return out.index_add_(0, seg, src)
def _pad(x, pw):
    flat = tuple(v for pair in reversed(pw) for v in pair)
    return torch.nn.functional.pad(x, flat)

kernel_name = 'interleave_layers__softmax_full_mha__glu_gated_swiglu__prompt'

def setup_inputs():

    _w = (lambda a: (lambda t: t.to(torch.bfloat16) if t.is_floating_point() else t)(torch.as_tensor(a)))
    def _A_setup():
        B, S, D = 1, 2048, 576
        return {
            "x": _w((lambda *s: torch.randn(*s, dtype=torch.bfloat16) * 0.05)(B, S, D)),
            "wqkv": _w((lambda *s: torch.randn(*s, dtype=torch.bfloat16) * 0.05)(D, 3 * D)),
            "wo": _w((lambda *s: torch.randn(*s, dtype=torch.bfloat16) * 0.05)(D, D)),
        }
    _A_in = _A_setup()

    def _B_setup():
        B, S, D, F = 1, 2048, 576, 4608
        return {
            "x": _w((lambda *s: torch.randn(*s, dtype=torch.bfloat16) * 0.05)(B, S, D)),
            "w1": _w((lambda *s: torch.randn(*s, dtype=torch.bfloat16) * 0.4)(D, F) * (2.0 / D) ** 0.5),
            "w3": _w((lambda *s: torch.randn(*s, dtype=torch.bfloat16) * 0.4)(D, F) * (2.0 / D) ** 0.5),
            "w2": _w((lambda *s: torch.randn(*s, dtype=torch.bfloat16) * 0.4)(F, D) * (2.0 / F) ** 0.5),
        }
    _B_in = _B_setup()

    return {
        "A_x": _A_in["x"],
        "A_wqkv": _A_in["wqkv"],
        "A_wo": _A_in["wo"],
        "B_x": _B_in["x"],
        "B_w1": _B_in["w1"],
        "B_w3": _B_in["w3"],
        "B_w2": _B_in["w2"],
    }

def reference(A_x, A_wqkv, A_wo, B_x, B_w1, B_w3, B_w2):
    def _A_ref(x, wqkv, wo):
        B, S, D = x.shape
        H = 32; Dh = D // H
        qkv = (lambda x, *d: x.permute(*d))((x @ wqkv).reshape(B, S, 3, H, Dh), 2, 0, 3, 1, 4)
        q, k, v = qkv[0], qkv[1], qkv[2]
        scale = Dh ** -0.5
        scores = torch.einsum("bhqd,bhkd->bhqk", q, k) * scale
        p = _softmax(scores, axis=-1)
        o = torch.einsum("bhqk,bhkd->bhqd", p, v)
        return (lambda x, *d: x.permute(*d))(o, 0, 2, 1, 3).reshape(B, S, D) @ wo

    def _B_ref(x, w1, w3, w2):
        gate = torch.einsum("bsd,df->bsf", x, w1)
        up = torch.einsum("bsd,df->bsf", x, w3)
        h = _silu(gate) * up
        out = torch.einsum("bsf,fd->bsd", h, w2)
        return out

    x = A_x
    x = x + _A_ref(_layernorm(x, axis=-1), A_wqkv, A_wo)
    x = x + _B_ref(_layernorm(x, axis=-1), B_w1, B_w3, B_w2)
    x = x + _A_ref(_layernorm(x, axis=-1), A_wqkv, A_wo)
    x = x + _B_ref(_layernorm(x, axis=-1), B_w1, B_w3, B_w2)
    return x
```

```cpp
#include <hip/hip_runtime.h>
#include <hip/hip_bf16.h>
#include <stdint.h>

typedef __bf16 bf16x8 __attribute__((ext_vector_type(8)));
typedef __bf16 bf16x2 __attribute__((ext_vector_type(2)));
typedef short s16x4 __attribute__((ext_vector_type(4)));
typedef float f32x16 __attribute__((ext_vector_type(16)));
typedef float f32x4 __attribute__((ext_vector_type(4)));
typedef float f32x2 __attribute__((ext_vector_type(2)));
typedef unsigned u32x4 __attribute__((ext_vector_type(4)));
typedef unsigned u32x2 __attribute__((ext_vector_type(2)));
#define LDSP __attribute__((address_space(3)))
typedef LDSP char* lds_ptr;

constexpr int S_ = 2048, D_ = 576, H_ = 32, F_ = 4608;
constexpr int KB = 1024;
constexpr size_t SZ_XT = 64 * 36 * KB;
constexpr size_t O_XT = 0;
constexpr size_t O_X1T = O_XT + SZ_XT;
constexpr size_t O_STA = O_X1T + SZ_XT;
constexpr size_t O_STB = O_STA + 65536;
constexpr size_t O_Q = O_STB + 65536;
constexpr size_t O_QX = O_Q + 32 * 64 * KB;
constexpr int KVG = 9728;
constexpr int KVH = 16 * KVG;
constexpr size_t O_KV = O_QX + 32 * 2048 * 4;
constexpr size_t O_OT = O_KV + (size_t)32 * KVH;
constexpr size_t O_HT = O_OT + 128 * 18 * KB;
constexpr size_t O_PART = O_HT + (size_t)128 * 144 * KB;
constexpr size_t O_WQ = O_PART + (size_t)4 * 128 * 36 * 512;
constexpr size_t O_WO = O_WQ + 16 * 144 * KB;
constexpr size_t O_W13 = O_WO + 36 * 18 * KB;
constexpr size_t O_W2 = O_W13 + (size_t)288 * 36 * KB;
constexpr int NCS = 2 * F_ + 3 * D_;
constexpr size_t O_CS = O_W2 + (size_t)4 * 144 * 9 * KB;

#define MFMA32(a, b, c) __builtin_amdgcn_mfma_f32_32x32x16_bf16(a, b, c, 0, 0, 0)
#define MFMA32K8(a, b, c) __builtin_amdgcn_mfma_f32_32x32x8bf16_1k(a, b, c, 0, 0, 0)
#define MFMA16(a, b, c) __builtin_amdgcn_mfma_f32_16x16x32_bf16(a, b, c, 0, 0, 0)
#define SBAR() __builtin_amdgcn_sched_barrier(0)
#define PIN(x) asm volatile("" : "+v"(x))

__device__ __forceinline__ unsigned pk2(float lo, float hi) {
    f32x2 v = {lo, hi};
    return __builtin_bit_cast(unsigned, __builtin_convertvector(v, bf16x2));
}
__device__ __forceinline__ float bflo(unsigned u) { return __uint_as_float(u << 16); }
__device__ __forceinline__ float bfhi(unsigned u) { return __uint_as_float(u & 0xffff0000u); }
__device__ __forceinline__ int swap23(int r) { return (r & ~12) | ((r & 4) << 1) | ((r & 8) >> 1); }
__device__ __forceinline__ int wave_id() { return __builtin_amdgcn_readfirstlane(threadIdx.x >> 6); }

__device__ __forceinline__ void dma1(const char* sb, unsigned voff, unsigned ld) {
    unsigned sv;
    asm volatile("s_mov_b32 %0, m0\n\ts_mov_b32 m0, %3\n\ts_nop 0\n\tglobal_load_lds_dwordx4 %1, %2\n\ts_mov_b32 m0, %0"
                 : "=&s"(sv) : "v"(voff), "s"(sb), "s"(ld) : "memory");
}
__device__ __forceinline__ void dma4(const char* sb, unsigned voff, unsigned ld) {
    unsigned sv;
    asm volatile("s_mov_b32 %0, m0\n\ts_mov_b32 m0, %3\n\ts_nop 0\n\tglobal_load_lds_dwordx4 %1, %2\n\t"
                 "global_load_lds_dwordx4 %1, %2 offset:1024\n\tglobal_load_lds_dwordx4 %1, %2 offset:2048\n\t"
                 "global_load_lds_dwordx4 %1, %2 offset:3072\n\ts_mov_b32 m0, %0"
                 : "=&s"(sv) : "v"(voff), "s"(sb), "s"(ld) : "memory");
}
__device__ __forceinline__ void gld4(bf16x8& a, bf16x8& b, bf16x8& c, bf16x8& d, const char* sb, unsigned voff) {
    asm volatile("global_load_dwordx4 %0, %4, %5\n\tglobal_load_dwordx4 %1, %4, %5 offset:1024\n\t"
                 "global_load_dwordx4 %2, %4, %5 offset:2048\n\tglobal_load_dwordx4 %3, %4, %5 offset:3072"
                 : "=&v"(a), "=&v"(b), "=&v"(c), "=&v"(d) : "v"(voff), "s"(sb) : "memory");
}
__device__ __forceinline__ void gld2(bf16x8& a, bf16x8& b, const char* sb, unsigned voff) {
    asm volatile("global_load_dwordx4 %0, %2, %3\n\tglobal_load_dwordx4 %1, %2, %3 offset:1024"
                 : "=&v"(a), "=&v"(b) : "v"(voff), "s"(sb) : "memory");
}
#define WAITV(N) asm volatile("s_waitcnt vmcnt(" #N ")" ::: "memory")
#define WAITV_BAR(N) asm volatile("s_waitcnt vmcnt(" #N ") lgkmcnt(0)\n\ts_barrier" ::: "memory")
#define WAITALL_BAR() asm volatile("s_waitcnt vmcnt(0) lgkmcnt(0)\n\ts_barrier" ::: "memory")
#define LDS_BAR() asm volatile("s_waitcnt lgkmcnt(0)\n\ts_barrier" ::: "memory")
__device__ __forceinline__ bf16x8 ldsfrag(lds_ptr p) { return *(LDSP bf16x8*)p; }

__device__ __forceinline__ void dma36(const char* src, unsigned ld, int w, unsigned l16) {
    dma4(src + w * 4 * KB, l16, ld + w * 4 * KB);
    if (w < 4) dma1(src + (32 + w) * KB, l16, ld + (32 + w) * KB);
}

__device__ __forceinline__ void row_stats(const float* st, int token, float& mu, float& rstd) {
    const f32x4 a = *(const f32x4*)(st + token * 8), b = *(const f32x4*)(st + token * 8 + 4);
    const float s1 = (a[0] + a[2]) + (b[0] + b[2]), s2 = (a[1] + a[3]) + (b[1] + b[3]);
    mu = s1 * (1.0f / D_);
    const float var = fmaxf(s2 * (1.0f / D_) - mu * mu, 0.f);
    rstd = __builtin_amdgcn_rsqf(var + 1e-5f);
}

constexpr int NJ_QKV = 9 * 27, NJ_WO = 9 * 9, NJ_W = 9 * 72;
constexpr int NJOBS = NJ_QKV + NJ_WO + 3 * NJ_W;

__device__ __forceinline__ void retile_job(int job, int lane, const uint16_t* wqkv, const uint16_t* wo, const uint16_t* w1,
                                           const uint16_t* w3, const uint16_t* w2, char* ws) {
    int mat, kb, cbk, ld;
    const uint16_t* src;
    if (job < NJ_QKV) { mat = 0; kb = job / 27; cbk = job % 27; ld = 3 * D_; src = wqkv; }
    else if (job < NJ_QKV + NJ_WO) { job -= NJ_QKV; mat = 1; kb = job / 9; cbk = job % 9; ld = D_; src = wo; }
    else if (job < NJ_QKV + NJ_WO + 2 * NJ_W) { job -= NJ_QKV + NJ_WO; mat = 2 + (job >= NJ_W); src = job >= NJ_W ? w3 : w1;
        if (job >= NJ_W) job -= NJ_W; kb = job / 72; cbk = job % 72; ld = F_; }
    else { job -= NJ_QKV + NJ_WO + 2 * NJ_W; mat = 4; kb = job / 9; cbk = job % 9; ld = D_; src = w2; }
    const int ko = kb * 8 + (lane >> 3), n0 = cbk * 64 + (lane & 7) * 8;
    u32x4 r[8];
#pragma unroll
    for (int j = 0; j < 8; ++j) {
        int row = ko * 8 + j;
        if (mat == 1) {
            const int kk = ko >> 2, g = ko & 3;
            row = kk < 16 ? 18 * (2 * kk + (g >> 1)) + 4 * (g & 1) + (j & 3) + 8 * (j >> 2)
                          : 18 * (16 * (kk - 16) + 4 * g + (j >> 1)) + 16 + (j & 1);
        }
        r[j] = *(const u32x4*)(src + (size_t)row * ld + n0);
    }
    float cs[8];
#pragma unroll
    for (int c = 0; c < 8; ++c) {
        u32x4 p;
#pragma unroll
        for (int m = 0; m < 4; ++m)
            p[m] = __builtin_amdgcn_perm(r[2 * m + 1][c >> 1], r[2 * m][c >> 1], (c & 1) ? 0x07060302u : 0x05040100u);
        cs[c] = ((bflo(p[0]) + bfhi(p[0])) + (bflo(p[1]) + bfhi(p[1]))) + ((bflo(p[2]) + bfhi(p[2])) + (bflo(p[3]) + bfhi(p[3])));
        const int n = n0 + c;
        size_t off;
        if (mat == 0) {
            const int slot = n / 18, dim = n - slot * 18, cb = slot / 6, sl = slot - cb * 6;
            int u, rho;
            if (dim < 16) { u = sl >> 1; rho = 16 * (sl & 1) + swap23(dim); }
            else { u = 3; const int hh = sl / 3, i = 2 * (sl - hh * 3) + dim - 16; rho = (i & 3) + 8 * (i >> 2) + 4 * hh; }
            off = O_WQ + ((size_t)((cb * 36 + (ko >> 1)) * 4 + u) * 64 + (ko & 1) * 32 + rho) * 16;
        } else if (mat == 1) {
            off = O_WO + ((size_t)((n >> 4) * 18 + (ko >> 2)) * 64 + (ko & 3) * 16 + (n & 15)) * 16;
        } else if (mat == 4) {
            const int db = n / 144, dt = (n - db * 144) >> 4;
            off = O_W2 + ((size_t)((db * 144 + (ko >> 2)) * 9 + dt) * 64 + (ko & 3) * 16 + (n & 15)) * 16;
        } else {
            off = O_W13 + ((size_t)((n >> 4) * 36 + (ko >> 1)) * 64 + (ko & 1) * 32 + swap23(n & 15) + (mat == 3 ? 16 : 0)) * 16;
        }
        *(u32x4*)(ws + off) = p;
    }
    if (mat == 0 || mat == 2 || mat == 3) {
#pragma unroll
        for (int c = 0; c < 8; ++c) {
            float v = cs[c];
            v += __shfl_xor(v, 8); v += __shfl_xor(v, 16); v += __shfl_xor(v, 32);
            cs[c] = v;
        }
        if (lane < 8) {
            float* dst = (float*)(ws + O_CS) + kb * NCS + (mat == 0 ? 2 * F_ : (mat == 3 ? F_ : 0)) + n0;
            *(f32x4*)dst = (f32x4){cs[0], cs[1], cs[2], cs[3]};
            *(f32x4*)(dst + 4) = (f32x4){cs[4], cs[5], cs[6], cs[7]};
        }
    }
}

template <bool FINAL>
__device__ __forceinline__ void finish_rows(const float (&v)[2][4], int tt, int db, int w, int lane, char* xt, float* st,
                                            uint16_t* out, float* red  ) {
    const int g = lane >> 4, c = lane & 15, dt = 9 * db + w;
    float s[4];
#pragma unroll
    for (int u = 0; u < 2; ++u) {
        const u32x2 p = {pk2(v[u][0], v[u][1]), pk2(v[u][2], v[u][3])};
        if (FINAL) { *(u32x2*)(out + (size_t)(32 * tt + 16 * u + c) * D_ + 16 * dt + 4 * g) = p; }
        else {
            *(u32x2*)(xt + ((size_t)(tt * 36 + dt) * 64 + (g >> 1) * 32 + 16 * u + c) * 16 + (g & 1) * 8) = p;
            const float a = bflo(p[0]), b = bfhi(p[0]), d = bflo(p[1]), e = bfhi(p[1]);
            s[2 * u] = (a + b) + (d + e);
            s[2 * u + 1] = (a * a + b * b) + (d * d + e * e);
        }
    }
    if (!FINAL) {
#pragma unroll
        for (int i = 0; i < 4; ++i) { s[i] += __shfl_xor(s[i], 16); s[i] += __shfl_xor(s[i], 32); }
        if (g < 2) *(f32x2*)(red + (w * 32 + 16 * g + c) * 2) = g ? (f32x2){s[2], s[3]} : (f32x2){s[0], s[1]};
        __syncthreads();
        if (threadIdx.x < 64) {
            float t = 0.f;
#pragma unroll
            for (int i = 0; i < 9; ++i) t += red[i * 64 + threadIdx.x];
            st[(32 * tt + (threadIdx.x >> 1)) * 8 + db * 2 + (threadIdx.x & 1)] = t;
        }
    }
}

__global__ __launch_bounds__(576) void k_prep(const uint16_t* x, const uint16_t* wqkv, const uint16_t* wo, const uint16_t* w1,
                                              const uint16_t* w3, const uint16_t* w2, char* ws) {
    __shared__ float red[9 * 64];
    const int w = wave_id(), lane = threadIdx.x & 63;
    if (blockIdx.x < 256) {
        const int tt = blockIdx.x >> 2, db = blockIdx.x & 3, g = lane >> 4, c = lane & 15, dt = 9 * db + w;
        float v[2][4];
#pragma unroll
        for (int u = 0; u < 2; ++u) {
            const u32x2 p = *(const u32x2*)(x + (size_t)(32 * tt + 16 * u + c) * D_ + 16 * dt + 4 * g);
            v[u][0] = bflo(p[0]); v[u][1] = bfhi(p[0]); v[u][2] = bflo(p[1]); v[u][3] = bfhi(p[1]);
        }
        finish_rows<false>(v, tt, db, w, lane, ws + O_XT, (float*)(ws + O_STA), nullptr, red);
    } else {
        const int job = (blockIdx.x - 256) * 9 + w;
        if (job < NJOBS) retile_job(job, lane, wqkv, wo, w1, w3, w2, ws);
    }
}

constexpr int QKV_LDS = 144 * KB + 512;
__global__ __launch_bounds__(256, 1) void k_qkv(char* ws) {
    extern __shared__ __attribute__((aligned(16))) char smem[];
    const int w = wave_id(), lane = threadIdx.x & 63, h = lane >> 5, c = lane & 31;
    const int xcd = blockIdx.x & 7, r = blockIdx.x >> 3;
    const int rb = (xcd & 3) * 4 + (r & 3), cb = (xcd >> 2) * 8 + (r >> 2), tt = rb * 4 + w;
    const unsigned l16 = lane * 16, lds0 = (unsigned)(uintptr_t)smem;
    const char* wsrc = ws + O_WQ + (size_t)cb * 144 * KB + w * 12 * KB;
    const char* xsrc = ws + O_XT + (size_t)tt * 36 * KB;
    bf16x8 xf[36];
#define QKV_ISSUE(ch) do { \
        dma4(wsrc + (ch) * 48 * KB, l16, lds0 + (ch) * 48 * KB + w * 12 * KB); \
        gld4(xf[12 * (ch)], xf[12 * (ch) + 1], xf[12 * (ch) + 2], xf[12 * (ch) + 3], xsrc + (12 * (ch)) * KB, l16); \
        dma4(wsrc + (ch) * 48 * KB + 4 * KB, l16, lds0 + (ch) * 48 * KB + w * 12 * KB + 4 * KB); \
        gld4(xf[12 * (ch) + 4], xf[12 * (ch) + 5], xf[12 * (ch) + 6], xf[12 * (ch) + 7], xsrc + (12 * (ch) + 4) * KB, l16); \
        dma4(wsrc + (ch) * 48 * KB + 8 * KB, l16, lds0 + (ch) * 48 * KB + w * 12 * KB + 8 * KB); \
        gld4(xf[12 * (ch) + 8], xf[12 * (ch) + 9], xf[12 * (ch) + 10], xf[12 * (ch) + 11], xsrc + (12 * (ch) + 8) * KB, l16); } while (0)
#define PIN12(b) asm volatile("" : "+v"(xf[b]), "+v"(xf[b + 1]), "+v"(xf[b + 2]), "+v"(xf[b + 3]), "+v"(xf[b + 4]), "+v"(xf[b + 5]), \
        "+v"(xf[b + 6]), "+v"(xf[b + 7]), "+v"(xf[b + 8]), "+v"(xf[b + 9]), "+v"(xf[b + 10]), "+v"(xf[b + 11]))
    float mu, rstd;
    row_stats((const float*)(ws + O_STA), tt * 32 + c, mu, rstd);
    float* csl = (float*)(smem + 144 * KB);
    if (threadIdx.x < 128) {
        const int u = threadIdx.x >> 5, rho = threadIdx.x & 31;
        int n;
        if (u < 3) n = (6 * cb + 2 * u + (rho >> 4)) * 18 + swap23(rho & 15);
        else { const int i = (rho & 3) + 4 * ((rho >> 3) & 1), hh = (rho >> 2) & 1; n = (6 * cb + 3 * hh + (i < 6 ? i >> 1 : 0)) * 18 + 16 + (i & 1); }
        const float* p = (const float*)(ws + O_CS) + 2 * F_ + n;
        float t = 0.f;
#pragma unroll
        for (int i = 0; i < 9; ++i) t += p[i * NCS];
        csl[threadIdx.x] = t;
    }
    PIN(mu); PIN(rstd);
    asm volatile("s_waitcnt vmcnt(0) lgkmcnt(0)" ::: "memory");
    QKV_ISSUE(0); QKV_ISSUE(1);
    f32x16 acc[4];
#pragma unroll
    for (int u = 0; u < 4; ++u) acc[u] = (f32x16){};
    const lds_ptr lp = (lds_ptr)smem + l16;
#define QKV_COMP(ch) do { _Pragma("unroll") for (int k = 0; k < 12; ++k) { _Pragma("unroll") for (int u = 0; u < 4; ++u) \
        acc[u] = MFMA32(ldsfrag(lp + (ch) * 48 * KB + (k * 4 + u) * KB), xf[12 * (ch) + k], acc[u]); } } while (0)
    WAITV_BAR(24); PIN12(0);
    QKV_ISSUE(2);
    QKV_COMP(0);
    WAITV_BAR(24); PIN12(12);
    QKV_COMP(1);
    WAITV_BAR(0); PIN12(24);
    QKV_COMP(2);
    const float qscale = 0.23570226039551584f * 1.4426950408889634f;
    const int token = tt * 32 + c;
    char* kvbase = ws + O_KV + (token >> 7) * KVG;
#pragma unroll
    for (int u = 0; u < 4; ++u) {
        float v[16];
#pragma unroll
        for (int m = 0; m < 4; ++m) {
            const f32x4 cs4 = *(const f32x4*)(csl + u * 32 + 8 * m + 4 * h);
#pragma unroll
            for (int i = 0; i < 4; ++i) v[4 * m + i] = (acc[u][4 * m + i] - mu * cs4[i]);
        }
        if (u < 3) {
            const int slot = 6 * cb + 2 * u, type = slot >> 5, head = slot & 31;
            const float sc = type == 0 ? rstd * qscale : rstd;
#pragma unroll
            for (int a = 0; a < 2; ++a) {
                const u32x4 p = {pk2(v[8 * a] * sc, v[8 * a + 1] * sc), pk2(v[8 * a + 2] * sc, v[8 * a + 3] * sc),
                                 pk2(v[8 * a + 4] * sc, v[8 * a + 5] * sc), pk2(v[8 * a + 6] * sc, v[8 * a + 7] * sc)};
                if (type == 0) *(u32x4*)(ws + O_Q + (size_t)((head + a) * 64 + tt) * KB + l16) = p;
                else if (type == 1) *(u32x4*)(kvbase + (size_t)(head + a) * KVH + (tt & 3) * KB + l16) = p;
                else {
                    char* d = kvbase + (size_t)(head + a) * KVH + 4608 + (token & 127) * 40 + 16 * h;
                    *(u32x2*)d = (u32x2){p[0], p[1]}; *(u32x2*)(d + 8) = (u32x2){p[2], p[3]};
                }
            }
        } else {
#pragma unroll
            for (int m = 0; m < 3; ++m) {
                const int slot = 6 * cb + 3 * h + m, type = slot >> 5, head = slot & 31;
                const float sc = type == 0 ? rstd * qscale : rstd;
                const unsigned p = pk2(v[2 * m] * sc, v[2 * m + 1] * sc);
                char* kvh = kvbase + (size_t)head * KVH;
                char* d = type == 0 ? ws + O_QX + (size_t)(head * 2048 + token) * 4
                        : type == 1 ? kvh + 4096 + (((tt >> 1) & 1) * 32 + c) * 8 + (tt & 1) * 4
                                    : kvh + 4608 + (token & 127) * 40 + 32;
                *(unsigned*)d = p;
                if (type == 2) *(unsigned*)(d + 4) = 0x00003F80u;
            }
        }
    }
}

typedef short v4i16_t __attribute__((ext_vector_type(4)));
__device__ __forceinline__ s16x4 vtr(lds_ptr p) { return __builtin_amdgcn_ds_read_tr16_b64_v4i16((LDSP v4i16_t*)p); }
__device__ __forceinline__ bf16x8 vfrag(lds_ptr p) {
    const s16x4 a = vtr(p), b = vtr(p + 320);
    typedef short s16x8 __attribute__((ext_vector_type(8)));
    const s16x8 r = {a[0], a[1], a[2], a[3], b[0], b[1], b[2], b[3]};
    return __builtin_bit_cast(bf16x8, r);
}
__device__ __forceinline__ bf16x8 packp(const f32x16& s, int b) {
    const u32x4 p = {pk2(s[b], s[b + 1]), pk2(s[b + 2], s[b + 3]), pk2(s[b + 4], s[b + 5]), pk2(s[b + 6], s[b + 7])};
    return __builtin_bit_cast(bf16x8, p);
}
__device__ __forceinline__ float halfmax(float m) {
    auto rr = __builtin_amdgcn_permlane32_swap(__float_as_uint(m), __float_as_uint(m), false, false);
    return fmaxf(__uint_as_float(rr[0]), __uint_as_float(rr[1]));
}
__device__ __forceinline__ float max16(const f32x16& s) {
    float m = s[0];
#pragma unroll
    for (int i = 1; i < 16; ++i) m = fmaxf(m, s[i]);
    return halfmax(m);
}

__global__ __launch_bounds__(512, 2) void k_att(char* ws) {
    extern __shared__ __attribute__((aligned(16))) char smem[];
    const int w = wave_id(), lane = threadIdx.x & 63, h = lane >> 5, c = lane & 31;
    const int head = 4 * (blockIdx.x & 7) + ((blockIdx.x >> 3) & 3), tt = (blockIdx.x >> 5) * 8 + w;
    const unsigned l16 = lane * 16, lds0 = (unsigned)(uintptr_t)smem;
    const bf16x8 qf = *(const bf16x8*)(ws + O_Q + (size_t)(head * 64 + tt) * KB + l16);
    unsigned qx = *(const unsigned*)(ws + O_QX + (size_t)(head * 2048 + tt * 32 + c) * 4);
    const char* kv = ws + O_KV + (size_t)head * KVH + w * KB;
#pragma unroll
    for (int i = 0; i < 19; ++i) dma1(kv + i * 8 * KB, l16, lds0 + w * KB + i * 8 * KB);
    s16x4 qv[4];
#pragma unroll
    for (int t = 0; t < 4; ++t) {
        const unsigned z = (h == (t >> 1)) ? qx : 0u;
        qv[t] = __builtin_bit_cast(s16x4, (t & 1) ? (u32x2){0u, z} : (u32x2){z, 0u});
    }
    const lds_ptr kp0 = (lds_ptr)smem + l16;
    const lds_ptr xp0 = (lds_ptr)smem + 4096 + lane * 8;
    const lds_ptr vp0 = (lds_ptr)smem + 4608 + (4 * h + ((lane & 15) >> 2)) * 40 + ((lane & 16) ? 32 : 8 * (lane & 3));
    WAITALL_BAR();
    f32x16 o = {}, cm, sA, sB;
    s16x4 kx = *(LDSP s16x4*)xp0;
    sA = MFMA32K8(kx, qv[0], o);
    sA = MFMA32(ldsfrag(kp0), qf, sA);
    {
        const float m0 = max16(sA);
#pragma unroll
        for (int i = 0; i < 16; ++i) { cm[i] = -m0; sA[i] -= m0; }
    }
#define ATT_STEP(SC, SN, KX, QV, KOFF, VOFF) do { \
        SN = MFMA32K8(KX, QV, cm); SN = MFMA32(ldsfrag(kp + (KOFF)), qf, SN); \
        const bf16x8 v0 = vfrag(vp + (VOFF)), v1 = vfrag(vp + (VOFF) + 640); \
        _Pragma("unroll") for (int i = 0; i < 8; ++i) SC[i] = __builtin_amdgcn_exp2f(SC[i]); \
        o = MFMA32(v0, packp(SC, 0), o); \
        _Pragma("unroll") for (int i = 8; i < 16; ++i) SC[i] = __builtin_amdgcn_exp2f(SC[i]); \
        o = MFMA32(v1, packp(SC, 8), o); } while (0)
    for (int g = 0; g < 16; ++g) {
        const lds_ptr kp = kp0 + g * KVG, vp = vp0 + g * KVG;
        const int gn = g < 15 ? KVG : 0;
        const s16x4 kxn = *(LDSP s16x4*)(xp0 + g * KVG + gn);
        ATT_STEP(sA, sB, kx, qv[1], 1024, 0);
        ATT_STEP(sB, sA, kx, qv[2], 2048, 1280);
        ATT_STEP(sA, sB, kx, qv[3], 3072, 2560);
        ATT_STEP(sB, sA, kxn, qv[0], gn, 3840);
        kx = kxn;
    }
    bool bad = !(o[10] < 1e30f);
#pragma unroll
    for (int i = 0; i < 10; ++i) bad |= !(fabsf(o[i]) < 3e38f);
    if (__builtin_expect(__any(bad), 0)) {
        float m = -INFINITY;
        o = (f32x16){};
        const f32x16 z = {};
        for (int t = 0; t < 64; ++t) {
            const int go = (t >> 2) * KVG, tl = t & 3;
            const unsigned zq = (h == (tl >> 1)) ? qx : 0u;
            const s16x4 qq = __builtin_bit_cast(s16x4, (tl & 1) ? (u32x2){0u, zq} : (u32x2){zq, 0u});
            f32x16 s = MFMA32K8(*(LDSP s16x4*)(xp0 + go), qq, z);
            s = MFMA32(ldsfrag(kp0 + go + tl * 1024), qf, s);
            const float mn = fmaxf(m, max16(s)), al = __builtin_amdgcn_exp2f(m - mn);
            m = mn;
#pragma unroll
            for (int i = 0; i < 16; ++i) { s[i] = __builtin_amdgcn_exp2f(s[i] - mn); o[i] *= al; }
            o = MFMA32(vfrag(vp0 + go + tl * 1280), packp(s, 0), o);
            o = MFMA32(vfrag(vp0 + go + tl * 1280 + 640), packp(s, 8), o);
        }
    }
    const float rl = 1.0f / o[10];
    const int tt16 = tt * 2 + (c >> 4);
    char* ot = ws + O_OT + (size_t)tt16 * 18 * KB + (c & 15) * 16;
    const u32x4 p = {pk2(o[0] * rl, o[1] * rl), pk2(o[2] * rl, o[3] * rl), pk2(o[4] * rl, o[5] * rl), pk2(o[6] * rl, o[7] * rl)};
    *(u32x4*)(ot + (head >> 1) * KB + (2 * (head & 1) + h) * 256) = p;
    if (h == 0) *(unsigned*)(ot + (16 + (head >> 4)) * KB + ((head >> 2) & 3) * 256 + (head & 3) * 4) = pk2(o[8] * rl, o[9] * rl);
}

__global__ __launch_bounds__(576) void k_wo(char* ws) {
    __shared__ __attribute__((aligned(16))) char smem[36 * KB + 9 * 64 * 4];
    const int w = wave_id(), lane = threadIdx.x & 63, g = lane >> 4, c = lane & 15;
    const int r = blockIdx.x >> 3, tt = 8 * (blockIdx.x & 7) + (r >> 2), db = r & 3, dt = 9 * db + w;
    const unsigned l16 = lane * 16, lds0 = (unsigned)(uintptr_t)smem;
    dma4(ws + O_OT + (size_t)tt * 36 * KB + w * 4 * KB, l16, lds0 + w * 4 * KB);
    bf16x8 wf[18];
#pragma unroll
    for (int k = 0; k < 18; ++k) wf[k] = *(const bf16x8*)(ws + O_WO + (size_t)(dt * 18 + k) * KB + l16);
    u32x2 xr[2];
#pragma unroll
    for (int u = 0; u < 2; ++u)
        xr[u] = *(const u32x2*)(ws + O_XT + ((size_t)(tt * 36 + dt) * 64 + (g >> 1) * 32 + 16 * u + c) * 16 + (g & 1) * 8);
    WAITV_BAR(20);
    f32x4 acc[2] = {};
    const lds_ptr lp = (lds_ptr)smem + l16;
#pragma unroll
    for (int k = 0; k < 18; ++k) {
        acc[0] = MFMA16(wf[k], ldsfrag(lp + k * KB), acc[0]);
        acc[1] = MFMA16(wf[k], ldsfrag(lp + (18 + k) * KB), acc[1]);
    }
    float v[2][4];
#pragma unroll
    for (int u = 0; u < 2; ++u) {
        v[u][0] = acc[u][0] + bflo(xr[u][0]); v[u][1] = acc[u][1] + bfhi(xr[u][0]);
        v[u][2] = acc[u][2] + bflo(xr[u][1]); v[u][3] = acc[u][3] + bfhi(xr[u][1]);
    }
    finish_rows<false>(v, tt, db, w, lane, ws + O_X1T, (float*)(ws + O_STB), nullptr, (float*)(smem + 36 * KB));
}

constexpr int SLOT = 36 * KB;
constexpr int FF_LDS = 4 * SLOT + 288 * 4;
__global__ __launch_bounds__(512, 2) void k_ff1(char* ws) {
    extern __shared__ __attribute__((aligned(16))) char smem[];
    const int w = wave_id(), lane = threadIdx.x & 63, h = lane >> 5, c = lane & 31;
    const int xcd = blockIdx.x & 7, r = blockIdx.x >> 3;
    const int rb = (xcd & 1) * 4 + (r & 3), cbk = (xcd >> 1) * 8 + (r >> 2), tt = rb * 8 + w, ft0 = cbk * 9;
    const unsigned l16 = lane * 16, lds0 = (unsigned)(uintptr_t)smem;
    const char* wsrc = ws + O_W13 + (size_t)ft0 * SLOT;
    const char* xsrc = ws + O_X1T + (size_t)tt * 36 * KB;
    float mu, rstd;
    row_stats((const float*)(ws + O_STB), tt * 32 + c, mu, rstd);
    float* csl = (float*)(smem + 4 * SLOT);
    if (threadIdx.x < 288) {
        const int t = threadIdx.x >> 5, rho = threadIdx.x & 31;
        const float* p = (const float*)(ws + O_CS) + (rho >> 4) * F_ + (ft0 + t) * 16 + swap23(rho & 15);
        float s = 0.f;
#pragma unroll
        for (int i = 0; i < 9; ++i) s += p[i * NCS];
        csl[threadIdx.x] = s;
    }
    PIN(mu); PIN(rstd);
    asm volatile("s_waitcnt vmcnt(0) lgkmcnt(0)" ::: "memory");
    bf16x8 xf[36];
    {
        const int t3 = w / 3, q3 = w - t3 * 3;
#pragma unroll
        for (int ch = 0; ch < 3; ++ch) {
            dma4(wsrc + t3 * SLOT + (ch * 12 + q3 * 4) * KB, l16, lds0 + t3 * SLOT + (ch * 12 + q3 * 4) * KB);
            if (w < 4) dma1(wsrc + 2 * SLOT + (ch * 12 + 8 + w) * KB, l16, lds0 + 2 * SLOT + (ch * 12 + 8 + w) * KB);
#pragma unroll
            for (int i = 0; i < 12; i += 4)
                gld4(xf[12 * ch + i], xf[12 * ch + i + 1], xf[12 * ch + i + 2], xf[12 * ch + i + 3], xsrc + (12 * ch + i) * KB, l16);
        }
        dma36(wsrc + 3 * SLOT, lds0 + 3 * SLOT, w, l16);
    }
    const lds_ptr lp = (lds_ptr)smem + l16;
    f32x16 a0 = {}, a1 = {}, a2 = {}, a3;
#define FF1_FIRST(ch) do { _Pragma("unroll") for (int k = 12 * (ch); k < 12 * (ch) + 12; ++k) { \
        a0 = MFMA32(ldsfrag(lp + k * KB), xf[k], a0); a1 = MFMA32(ldsfrag(lp + SLOT + k * KB), xf[k], a1); \
        a2 = MFMA32(ldsfrag(lp + 2 * SLOT + k * KB), xf[k], a2); } } while (0)
    WAITV_BAR(36); PIN12(0);
    FF1_FIRST(0);
    WAITV_BAR(20); PIN12(12);
    FF1_FIRST(1);
    WAITV_BAR(4); PIN12(24);
    FF1_FIRST(2);
    const float nrl = -rstd * 1.4426950408889634f, r2 = rstd * rstd;
    char* hdst = ws + O_HT + (size_t)(tt * 2 + (c >> 4)) * 144 * KB + (c & 15) * 16 + h * 256;
#define FF1_EPI(A, t) do { float hv[8]; \
        _Pragma("unroll") for (int m = 0; m < 2; ++m) { \
            const f32x4 cg = *(const f32x4*)(csl + (t) * 32 + 8 * m + 4 * h), cu = *(const f32x4*)(csl + (t) * 32 + 16 + 8 * m + 4 * h); \
            _Pragma("unroll") for (int i = 0; i < 4; ++i) { \
                const float tg = A[4 * m + i] - mu * cg[i], tu = A[8 + 4 * m + i] - mu * cu[i]; \
                const float e = __builtin_amdgcn_exp2f(tg * nrl); \
                hv[4 * m + i] = (tg * tu) * (r2 * __builtin_amdgcn_rcpf(1.0f + e)); } } \
        const u32x4 p = {pk2(hv[0], hv[1]), pk2(hv[2], hv[3]), pk2(hv[4], hv[5]), pk2(hv[6], hv[7])}; \
        const int ft = ft0 + (t); \
        *(u32x4*)(hdst + (ft >> 1) * KB + (ft & 1) * 512) = p; } while (0)
#define FF1_TILE(A, slot) do { A = (f32x16){}; _Pragma("unroll") for (int k = 0; k < 36; ++k) \
        A = MFMA32(ldsfrag(lp + (slot) * SLOT + k * KB), xf[k], A); } while (0)
    WAITV_BAR(0);
    dma36(wsrc + 4 * SLOT, lds0, w, l16); dma36(wsrc + 5 * SLOT, lds0 + SLOT, w, l16); dma36(wsrc + 6 * SLOT, lds0 + 2 * SLOT, w, l16);
    FF1_TILE(a3, 3); FF1_EPI(a0, 0); FF1_EPI(a1, 1);
    WAITV_BAR(10);
    dma36(wsrc + 7 * SLOT, lds0 + 3 * SLOT, w, l16);
    FF1_TILE(a0, 0); FF1_EPI(a2, 2); FF1_EPI(a3, 3);
    WAITV_BAR(12);
    dma36(wsrc + 8 * SLOT, lds0, w, l16);
    FF1_TILE(a1, 1); FF1_EPI(a0, 4);
    WAITV_BAR(13);
    FF1_TILE(a2, 2); FF1_EPI(a1, 5);
    WAITV_BAR(8);
    FF1_TILE(a3, 3); FF1_EPI(a2, 6);
    WAITV_BAR(3);
    FF1_TILE(a0, 0); FF1_EPI(a3, 7);
    FF1_EPI(a0, 8);
}

__global__ __launch_bounds__(512, 2) void k_ff2(char* ws) {
    extern __shared__ __attribute__((aligned(16))) char smem[];
    const int w = wave_id(), lane = threadIdx.x & 63;
    const int xcd = blockIdx.x & 7, r = blockIdx.x >> 3;
    const int rb = (xcd & 1) * 8 + (r >> 2), db = r & 3, q = xcd >> 1, tg = w & 3, kh = w >> 2;
    const unsigned l16 = lane * 16, lds0 = (unsigned)(uintptr_t)smem;
    const char* wsrc = ws + O_W2 + (size_t)(db * 144 + 36 * q) * 9 * KB;
    const int t16 = rb * 8 + tg * 2;
    const char* h0 = ws + O_HT + ((size_t)t16 * 144 + 36 * q + 2 * kh) * KB;
    const char* h1 = h0 + 144 * KB;
    bf16x8 hf[4][4];
#define FF2_ISSUE(ch) do { dma36(wsrc + (ch) * SLOT, lds0 + ((ch) & 3) * SLOT, w, l16); \
        gld2(hf[(ch) & 3][0], hf[(ch) & 3][1], h0 + (ch) * 4 * KB, l16); gld2(hf[(ch) & 3][2], hf[(ch) & 3][3], h1 + (ch) * 4 * KB, l16); } while (0)
    FF2_ISSUE(0); FF2_ISSUE(1); FF2_ISSUE(2);
    f32x4 acc[2][9];
#pragma unroll
    for (int i = 0; i < 9; ++i) { acc[0][i] = (f32x4){}; acc[1][i] = (f32x4){}; }
    const lds_ptr lp = (lds_ptr)smem + l16 + kh * 18 * KB;
#define FF2_STEP(ch, N) do { WAITV_BAR(N); \
        asm volatile("" : "+v"(hf[(ch) & 3][0]), "+v"(hf[(ch) & 3][1]), "+v"(hf[(ch) & 3][2]), "+v"(hf[(ch) & 3][3])); \
        if ((ch) + 3 < 9) FF2_ISSUE((ch) + 3); \
        _Pragma("unroll") for (int k = 0; k < 2; ++k) _Pragma("unroll") for (int i = 0; i < 9; ++i) { \
            const bf16x8 a = ldsfrag(lp + ((ch) & 3) * SLOT + (k * 9 + i) * KB); \
            acc[0][i] = MFMA16(a, hf[(ch) & 3][k], acc[0][i]); acc[1][i] = MFMA16(a, hf[(ch) & 3][2 + k], acc[1][i]); } } while (0)
    FF2_STEP(0, 16); FF2_STEP(1, 16); FF2_STEP(2, 16); FF2_STEP(3, 16); FF2_STEP(4, 16); FF2_STEP(5, 16); FF2_STEP(6, 16);
    FF2_STEP(7, 8); FF2_STEP(8, 0);
    LDS_BAR();
    {
        LDSP f32x4* xo = (LDSP f32x4*)((lds_ptr)smem + w * 9 * KB + l16);
#pragma unroll
        for (int i = 0; i < 9; ++i) xo[i * 64] = kh ? acc[0][i] : acc[1][i];
    }
    LDS_BAR();
    const LDSP f32x4* xi = (const LDSP f32x4*)((lds_ptr)smem + (w ^ 4) * 9 * KB + l16);
    char* dst = ws + O_PART + ((size_t)(q * 128 + t16 + kh) * 36 + db * 9) * 512 + lane * 8;
#pragma unroll
    for (int i = 0; i < 9; ++i) {
        const f32x4 s = xi[i * 64] + (kh ? acc[1][i] : acc[0][i]);
        *(u32x2*)(dst + i * 512) = (u32x2){pk2(s[0], s[1]), pk2(s[2], s[3])};
    }
}

template <bool FINAL>
__global__ __launch_bounds__(576) void k_red(char* ws, uint16_t* out) {
    __shared__ float red[9 * 64];
    const int w = wave_id(), lane = threadIdx.x & 63, g = lane >> 4, c = lane & 15;
    const int tt = blockIdx.x >> 2, db = blockIdx.x & 3, dt = 9 * db + w;
    float v[2][4];
#pragma unroll
    for (int u = 0; u < 2; ++u) {
        const u32x2 x = *(const u32x2*)(ws + O_X1T + ((size_t)(tt * 36 + dt) * 64 + (g >> 1) * 32 + 16 * u + c) * 16 + (g & 1) * 8);
        u32x2 p[4];
#pragma unroll
        for (int q = 0; q < 4; ++q) p[q] = *(const u32x2*)(ws + O_PART + ((size_t)(q * 128 + 2 * tt + u) * 36 + dt) * 512 + lane * 8);
        v[u][0] = bflo(x[0]) + ((bflo(p[0][0]) + bflo(p[1][0])) + (bflo(p[2][0]) + bflo(p[3][0])));
        v[u][1] = bfhi(x[0]) + ((bfhi(p[0][0]) + bfhi(p[1][0])) + (bfhi(p[2][0]) + bfhi(p[3][0])));
        v[u][2] = bflo(x[1]) + ((bflo(p[0][1]) + bflo(p[1][1])) + (bflo(p[2][1]) + bflo(p[3][1])));
        v[u][3] = bfhi(x[1]) + ((bfhi(p[0][1]) + bfhi(p[1][1])) + (bfhi(p[2][1]) + bfhi(p[3][1])));
    }
    finish_rows<FINAL>(v, tt, db, w, lane, ws + O_XT, (float*)(ws + O_STA), out, red);
}

extern "C" void kernel_launch(void* const* d_in, const int* in_sizes, int n_in, void* d_out, int out_size, void* d_ws,
                              size_t ws_size, hipStream_t stream) {
    const uint16_t* x = (const uint16_t*)d_in[0];
    const uint16_t* wqkv = (const uint16_t*)d_in[1];
    const uint16_t* wo = (const uint16_t*)d_in[2];
    const uint16_t* w1 = (const uint16_t*)d_in[4];
    const uint16_t* w3 = (const uint16_t*)d_in[5];
    const uint16_t* w2 = (const uint16_t*)d_in[6];
    char* ws = (char*)d_ws;
    uint16_t* out = (uint16_t*)d_out;
    (void)hipFuncSetAttribute((const void*)k_qkv, hipFuncAttributeMaxDynamicSharedMemorySize, QKV_LDS);
    (void)hipFuncSetAttribute((const void*)k_att, hipFuncAttributeMaxDynamicSharedMemorySize, KVH);
    (void)hipFuncSetAttribute((const void*)k_ff1, hipFuncAttributeMaxDynamicSharedMemorySize, FF_LDS);
    (void)hipFuncSetAttribute((const void*)k_ff2, hipFuncAttributeMaxDynamicSharedMemorySize, FF_LDS);
    k_prep<<<256 + (NJOBS + 8) / 9, 576, 0, stream>>>(x, wqkv, wo, w1, w3, w2, ws);
    for (int layer = 0; layer < 2; ++layer) {
        k_qkv<<<256, 256, QKV_LDS, stream>>>(ws);
        k_att<<<256, 512, KVH, stream>>>(ws);
        k_wo<<<256, 576, 0, stream>>>(ws);
        k_ff1<<<256, 512, FF_LDS, stream>>>(ws);
        k_ff2<<<256, 512, FF_LDS, stream>>>(ws);
        if (layer == 0) k_red<false><<<256, 576, 0, stream>>>(ws, out);
        else k_red<true><<<256, 576, 0, stream>>>(ws, out);
    }
}
```

```cpp
#include <hip/hip_runtime.h>
#include <hip/hip_bf16.h>
#include <stdint.h>
#include <utility>
#include <type_traits>

typedef __bf16 bf16x8 __attribute__((ext_vector_type(8)));
typedef __bf16 bf16x2 __attribute__((ext_vector_type(2)));
typedef short s16x4 __attribute__((ext_vector_type(4)));
typedef float f32x16 __attribute__((ext_vector_type(16)));
typedef float f32x4 __attribute__((ext_vector_type(4)));
typedef float f32x2 __attribute__((ext_vector_type(2)));
typedef unsigned u32x4 __attribute__((ext_vector_type(4)));
typedef unsigned u32x2 __attribute__((ext_vector_type(2)));
#define LDSP __attribute__((address_space(3)))
typedef LDSP char* lds_ptr;

constexpr int S_ = 2048, D_ = 576, H_ = 32, F_ = 4608;
constexpr int KB = 1024;
constexpr size_t SZ_XT = 64 * 36 * KB;
constexpr size_t O_XT = 0;
constexpr size_t O_X1T = O_XT + SZ_XT;
constexpr size_t O_STA = O_X1T + SZ_XT;
constexpr size_t O_STB = O_STA + 65536;
constexpr size_t O_Q = O_STB + 65536;
constexpr size_t O_QX = O_Q + 32 * 64 * KB;
constexpr int KVG = 9728;
constexpr int KVH = 16 * KVG;
constexpr size_t O_KV = O_QX + 32 * 2048 * 4;
constexpr size_t O_OT = O_KV + (size_t)32 * KVH;
constexpr size_t O_HT = O_OT + SZ_XT;
constexpr size_t O_PART = O_HT + (size_t)128 * 144 * KB;
constexpr size_t O_WQ = O_PART + (size_t)4 * 128 * 36 * 512;
constexpr size_t O_WO = O_WQ + 16 * 144 * KB;
constexpr size_t O_W13 = O_WO + 36 * 18 * KB;
constexpr size_t O_W2 = O_W13 + (size_t)288 * 36 * KB;
constexpr int NCS = 2 * F_ + 3 * D_;
constexpr size_t O_CS = O_W2 + (size_t)4 * 144 * 9 * KB;

#define STAMPR(i)
#define STAMP_DECL
#define STAMP_OUT(slot, n)
#define MFMA32(a, b, c) __builtin_amdgcn_mfma_f32_32x32x16_bf16(a, b, c, 0, 0, 0)
#define MFMA32K8(a, b, c) __builtin_amdgcn_mfma_f32_32x32x8bf16_1k(a, b, c, 0, 0, 0)
#define MFMA16(a, b, c) __builtin_amdgcn_mfma_f32_16x16x32_bf16(a, b, c, 0, 0, 0)
#define SBAR() __builtin_amdgcn_sched_barrier(0)
#define PIN(x) asm volatile("" : "+v"(x))

__device__ __forceinline__ unsigned pk2(float lo, float hi) {
    f32x2 v = {lo, hi};
    return __builtin_bit_cast(unsigned, __builtin_convertvector(v, bf16x2));
}
__device__ __forceinline__ float bflo(unsigned u) { return __uint_as_float(u << 16); }
__device__ __forceinline__ float bfhi(unsigned u) { return __uint_as_float(u & 0xffff0000u); }
__device__ __forceinline__ int swap23(int r) { return (r & ~12) | ((r & 4) << 1) | ((r & 8) >> 1); }
__device__ __forceinline__ int wave_id() { return __builtin_amdgcn_readfirstlane(threadIdx.x >> 6); }

__device__ __forceinline__ void dma1(const char* sb, unsigned voff, unsigned ld) {
    unsigned sv;
    asm volatile("s_mov_b32 %0, m0\n\ts_mov_b32 m0, %3\n\ts_nop 0\n\tglobal_load_lds_dwordx4 %1, %2\n\ts_mov_b32 m0, %0"
                 : "=&s"(sv) : "v"(voff), "s"(sb), "s"(ld) : "memory");
}
__device__ __forceinline__ void dma4(const char* sb, unsigned voff, unsigned ld) {
    unsigned sv;
    asm volatile("s_mov_b32 %0, m0\n\ts_mov_b32 m0, %3\n\ts_nop 0\n\tglobal_load_lds_dwordx4 %1, %2\n\t"
                 "global_load_lds_dwordx4 %1, %2 offset:1024\n\tglobal_load_lds_dwordx4 %1, %2 offset:2048\n\t"
                 "global_load_lds_dwordx4 %1, %2 offset:3072\n\ts_mov_b32 m0, %0"
                 : "=&s"(sv) : "v"(voff), "s"(sb), "s"(ld) : "memory");
}
#define WAITV(N) asm volatile("s_waitcnt vmcnt(" #N ")" ::: "memory")
#define WAITV_BAR(N) asm volatile("s_waitcnt vmcnt(" #N ") lgkmcnt(0)\n\ts_barrier" ::: "memory")
#define WAITALL_BAR() asm volatile("s_waitcnt vmcnt(0) lgkmcnt(0)\n\ts_barrier" ::: "memory")
#define LDS_BAR() asm volatile("s_waitcnt lgkmcnt(0)\n\ts_barrier" ::: "memory")
__device__ __forceinline__ bf16x8 ldsfrag(lds_ptr p) { return *(LDSP bf16x8*)p; }

#define IC(n) std::integral_constant<int, n>{}
#define PF_CODE(tmp, SYM, BYTES) PF_CODEW(0, tmp, SYM, BYTES)
#define PF_CODEW(W, tmp, SYM, BYTES) do { if (w == W) { unsigned long long pc_; \
        asm volatile("s_getpc_b64 vcc\n\ts_add_u32 vcc_lo, vcc_lo, " SYM "@rel32@lo+4\n\ts_addc_u32 vcc_hi, vcc_hi, " SYM "@rel32@hi+12\n\ts_mov_b64 %0, vcc" : "=s"(pc_) :: "scc", "vcc"); \
        const unsigned o_ = (((blockIdx.x >> 3) & 3) * 64 + lane) * 128; \
        asm volatile("global_load_dword %0, %1, %2" : "+v"(tmp) : "v"(o_ < (BYTES) ? o_ : 0u), "s"(pc_) : "memory"); } } while (0)
#define PF_DONE(tmp) asm volatile("s_waitcnt vmcnt(0)" :: "v"(tmp) : "memory")
template <class T> __device__ __forceinline__ void pinv(T& x) { asm volatile("" : "+v"(x)); }
template <class F, int... I> __device__ __forceinline__ void sfor_(F&& f, std::integer_sequence<int, I...>) { (f(std::integral_constant<int, I>{}), ...); }
template <int N, class F> __device__ __forceinline__ void sfor(F&& f) { sfor_(f, std::make_integer_sequence<int, N>{}); }
template <int N> __device__ __forceinline__ void waitv_bar() { asm volatile("s_waitcnt vmcnt(%0)\n\ts_barrier" :: "n"(N) : "memory"); }
template <int N> __device__ __forceinline__ void waitvl_bar() { asm volatile("s_waitcnt vmcnt(%0) lgkmcnt(0)\n\ts_barrier" :: "n"(N) : "memory"); }
__device__ __forceinline__ void gldf(float& a, const char* sb, unsigned voff) {
    asm volatile("global_load_dword %0, %1, %2" : "=&v"(a) : "v"(voff), "s"(sb) : "memory");
}
__device__ __forceinline__ void gld_stats(f32x4& a, f32x4& b, const char* sb, unsigned voff) {
    asm volatile("global_load_dwordx4 %0, %2, %3\n\tglobal_load_dwordx4 %1, %2, %3 offset:16" : "=&v"(a), "=&v"(b) : "v"(voff), "s"(sb) : "memory");
}
__device__ __forceinline__ void stats_of(const f32x4& a, const f32x4& b, float& mu, float& rstd) {
    const float s1 = (a[0] + a[2]) + (b[0] + b[2]), s2 = (a[1] + a[3]) + (b[1] + b[3]);
    mu = s1 * (1.0f / D_);
    rstd = __builtin_amdgcn_rsqf(fmaxf(s2 * (1.0f / D_) - mu * mu, 0.f) + 1e-5f);
}

template <int N> __device__ __forceinline__ void waitv() { asm volatile("s_waitcnt vmcnt(%0)" :: "n"(N) : "memory"); }
template <int OFF> __device__ __forceinline__ void gldo(bf16x8& a, const char* sb, unsigned voff) {
    asm volatile("global_load_dwordx4 %0, %1, %2 offset:%3" : "=&v"(a) : "v"(voff), "s"(sb), "n"(OFF) : "memory");
}
#define H_POL " sc1"
template <int OFF> __device__ __forceinline__ void gst(char* sb, unsigned voff, u32x4 v) {
    asm volatile("global_store_dwordx4 %0, %1, %2 offset:%3" H_POL "\n\ts_nop 1" :: "v"(voff), "v"(v), "s"(sb), "n"(OFF) : "memory");
}
constexpr unsigned EMPTY = 0xffffffffu;
constexpr int SPIN_CAP = 1 << 17;
__device__ __forceinline__ unsigned pk2s(float lo, float hi) { return min(pk2(lo, hi), EMPTY - 1); }
__device__ __forceinline__ unsigned umax4(const u32x4& a) { return max(max(a[0], a[1]), max(a[2], a[3])); }
__device__ __forceinline__ u32x4 swap_pair(const u32x4& x) {
    const auto a = __builtin_amdgcn_permlane16_swap(x[0], x[2], false, false), b = __builtin_amdgcn_permlane16_swap(x[1], x[3], false, false);
    return (u32x4){a[0], b[0], a[1], b[1]};
}
__device__ __forceinline__ f32x4 unpk(unsigned a, unsigned b) { return (f32x4){bflo(a), bfhi(a), bflo(b), bfhi(b)}; }
__device__ __forceinline__ void stat_add(unsigned o, float& a, float& b) { const float l = bflo(o), h = bfhi(o); a += l + h; b += l * l + h * h; }
__device__ __forceinline__ float stat_red(float a, float b) {
    const auto t = __builtin_amdgcn_permlane32_swap(__float_as_uint(a), __float_as_uint(b), false, false);
    const unsigned u = __float_as_uint(__uint_as_float(t[0]) + __uint_as_float(t[1]));
    const auto r = __builtin_amdgcn_permlane16_swap(u, u, false, false);
    return __uint_as_float(r[0]) + __uint_as_float(r[1]);
}
__device__ __forceinline__ void mf16(f32x4& c, const bf16x8& a, const bf16x8& b) { asm volatile("v_mfma_f32_16x16x32_bf16 %0, %1, %2, %0" : "+v"(c) : "v"(a), "v"(b)); }
__device__ __forceinline__ void mf16z(f32x4& c, const bf16x8& a, const bf16x8& b) { asm volatile("v_mfma_f32_16x16x32_bf16 %0, %1, %2, 0" : "=&v"(c) : "v"(a), "v"(b)); }

constexpr int NJ_QKV = 9 * 27, NJ_WO = 9 * 9, NJ_W = 9 * 72;
__device__ __forceinline__ u32x4 col_of(const u32x4 (&r)[8], int c) {
    u32x4 p;
#pragma unroll
    for (int m = 0; m < 4; ++m) p[m] = __builtin_amdgcn_perm(r[2 * m + 1][c >> 1], r[2 * m][c >> 1], (c & 1) ? 0x07060302u : 0x05040100u);
    return p;
}
__device__ __forceinline__ float sum8(const u32x4& p, float t) {
    const bf16x2 one = {(__bf16)1.0f, (__bf16)1.0f};
#pragma unroll
    for (int m = 0; m < 4; ++m) { const unsigned e = p[m]; t = __builtin_amdgcn_fdot2_f32_bf16(__builtin_bit_cast(bf16x2, e), one, t, false); }
    return t;
}
__device__ __forceinline__ void colsum_store(const float (&cs)[8], int lane, float* dst) {
    float t[4], u[2];
#pragma unroll
    for (int i = 0; i < 4; ++i) {
        const auto x = __builtin_amdgcn_permlane32_swap(__float_as_uint(cs[i]), __float_as_uint(cs[i + 4]), false, false);
        t[i] = __uint_as_float(x[0]) + __uint_as_float(x[1]);
    }
#pragma unroll
    for (int i = 0; i < 2; ++i) {
        const auto x = __builtin_amdgcn_permlane16_swap(__float_as_uint(t[i]), __float_as_uint(t[i + 2]), false, false);
        u[i] = __uint_as_float(x[0]) + __uint_as_float(x[1]);
        u[i] += __uint_as_float(__builtin_amdgcn_update_dpp(0u, __float_as_uint(u[i]), 0x128, 0xf, 0xf, false));
    }
    if (!(lane & 8)) *(f32x2*)(dst + (lane & 7) * 8 + ((lane >> 4) & 1) * 2 + (lane >> 5) * 4) = (f32x2){u[0], u[1]};
}
__device__ __forceinline__ void retile_qkv(int job, int lane, const uint16_t* wqkv, char* ws) {
    const int kb = job / 27, cbk = job % 27;
    const int ko = kb * 8 + (lane >> 3), n0 = cbk * 64 + (lane & 7) * 8;
    u32x4 r[8];
#pragma unroll
    for (int j = 0; j < 8; ++j) r[j] = *(const u32x4*)(wqkv + (size_t)(ko * 8 + j) * (3 * D_) + n0);
    float cs[8];
#pragma unroll
    for (int c = 0; c < 8; ++c) {
        const u32x4 p = col_of(r, c);
        cs[c] = sum8(p, 0.f);
        const int n = n0 + c, slot = n / 18, dim = n - slot * 18, cb = slot / 6, sl = slot - cb * 6;
        int u, rho;
        if (dim < 16) { u = sl >> 1; rho = 16 * (sl & 1) + swap23(dim); }
        else { u = 3; const int hh = sl / 3, i = 2 * (sl - hh * 3) + dim - 16; rho = (i & 3) + 8 * (i >> 2) + 4 * hh; }
        *(u32x4*)(ws + O_WQ + ((size_t)((cb * 36 + (ko >> 1)) * 4 + u) * 64 + (ko & 1) * 32 + rho) * 16) = p;
    }
    colsum_store(cs, lane, (float*)(ws + O_CS) + kb * NCS + 2 * F_ + cbk * 64);
}
__device__ __forceinline__ void retile_wo(int job, int lane, const uint16_t* wo, char* ws) {
    const int kb = job / 9, cbk = job % 9, jk = kb * 8 + (lane >> 3), n0 = cbk * 64 + (lane & 7) * 8;
    u32x4 r[8];
#pragma unroll
    for (int j = 0; j < 8; ++j)
        r[j] = *(const u32x4*)(wo + (size_t)(kb < 8 ? 18 * (jk >> 1) + 4 * (jk & 1) + (j & 3) + 8 * (j >> 2) : 72 * (jk - 64) + 16 + 18 * (j >> 1) + (j & 1)) * D_ + n0);
#pragma unroll
    for (int c = 0; c < 8; ++c) *(u32x4*)(ws + O_WO + (size_t)((n0 >> 4) * 18 + (jk >> 2)) * KB + ((jk & 3) * 16 + (n0 & 15) + c) * 16) = col_of(r, c);
}
struct FfnJob { int m, kb, cbk; };
__device__ __forceinline__ FfnJob ffn_job(int j) {
    if (j >= 3 * NJ_W) j -= 2048 - 3 * NJ_W;
    const int m = j / NJ_W, jj = j - m * NJ_W, kb = m < 2 ? jj / 72 : jj / 9;
    return {m, kb, jj - kb * (m < 2 ? 72 : 9)};
}

__global__ __launch_bounds__(192) void k_prep(const uint16_t* x, const uint16_t* wqkv, const uint16_t* wo, char* ws) {
    STAMP_DECL; STAMPR(0);
    const int w = wave_id(), lane = threadIdx.x & 63;
    if (w == 0) {
        const int tt = blockIdx.x >> 2, db = blockIdx.x & 3, row = 32 * tt + (lane & 31);
        u32x4 v[9];
#pragma unroll
        for (int i = 0; i < 9; ++i) v[i] = *(const u32x4*)(x + (size_t)row * D_ + db * 144 + 16 * i + (lane >> 5) * 8);
        unsigned pf = 0;
        PF_CODE(pf, "_Z5k_qkvPc", 22 * KB);
        float a = 0.f, b = 0.f;
#pragma unroll
        for (int i = 0; i < 9; ++i) {
            gst<0>(ws + O_XT + (size_t)(tt * 36 + 9 * db + i) * KB, lane * 16, v[i]);
#pragma unroll
            for (int m = 0; m < 4; ++m) stat_add(v[i][m], a, b);
        }
        const auto t = __builtin_amdgcn_permlane32_swap(__float_as_uint(a), __float_as_uint(b), false, false);
        ((float*)(ws + O_STA))[row * 8 + db * 2 + (lane >> 5)] = __uint_as_float(t[0]) + __uint_as_float(t[1]);
        PF_DONE(pf);
    } else if (w == 1) {
        if (blockIdx.x < NJ_QKV) retile_qkv(blockIdx.x, lane, wqkv, ws);
    } else if (blockIdx.x >= 256 - NJ_WO) retile_wo(blockIdx.x - (256 - NJ_WO), lane, wo, ws);
    STAMPR(1); STAMP_OUT(7, 2);
}

constexpr int QKV_LDS = 144 * KB + 512 + 4 * 256;
template <int NV> __device__ __forceinline__ void qkv_body(char* ws, char* smem, int w, int lane, int tt, int cb) {
    STAMP_DECL; STAMPR(0);
    const int h = lane >> 5, c = lane & 31;
    const unsigned l16 = lane * 16, lds0 = (unsigned)(uintptr_t)smem;
    const char* wsrc = ws + O_WQ + (size_t)cb * 144 * KB + w * KB;
    const char* xsrc = ws + O_XT + (size_t)tt * 36 * KB;
    bf16x8 xf[36];
    f32x4 sa, sb;
    float cp[9];
    auto wld = [&](int k) { dma1(wsrc + k * 4 * KB, l16, lds0 + (k * 4 + w) * KB); };
    auto xld = [&](auto K) { constexpr int k = K; gldo<(k & 3) * KB>(xf[k], xsrc + (k & ~3) * KB, l16); };
    wld(0); xld(IC(0)); wld(1); xld(IC(1));
    asm volatile("s_barrier" ::: "memory");
    gld_stats(sa, sb, ws + O_STA + tt * 1024, c * 32);
    if (w < 2) {
        const int u = threadIdx.x >> 5, rho = c;
        int n;
        if (u < 3) n = (6 * cb + 2 * u + (rho >> 4)) * 18 + swap23(rho & 15);
        else { const int i = (rho & 3) + 4 * ((rho >> 3) & 1), hh = (rho >> 2) & 1; n = (6 * cb + 3 * hh + (i < 6 ? i >> 1 : 0)) * 18 + 16 + (i & 1); }
#pragma unroll
        for (int i = 0; i < 9; ++i) gldf(cp[i], ws + O_CS + (i * NCS + 2 * F_) * 4, n * 4);
    }
    sfor<4>([&](auto J) { wld(2 + J); xld(std::integral_constant<int, 2 + J>{}); });
    asm volatile("s_barrier" ::: "memory");
    sfor<4>([&](auto J) { wld(6 + J); xld(std::integral_constant<int, 6 + J>{}); });
    unsigned lb[3] = {lds0 + l16, lds0 + l16 + 64 * KB, lds0 + l16 + 128 * KB};
    PIN(lb[0]); PIN(lb[1]); PIN(lb[2]);
    auto wfrag = [&](int p) { return *(const LDSP bf16x8*)(uintptr_t)(lb[p >> 6] + (p & 63) * KB); };
    LDSP float* csl = (LDSP float*)((lds_ptr)smem + 144 * KB);
    LDSP float* stl = csl + 128 + w * 64;
    f32x16 acc[4];
    bf16x8 fr[4];
    float mu, rstd;
    STAMPR(1);
    waitv_bar<18>(); PIN(xf[0]); PIN(xf[1]);
    STAMPR(2);
    fr[0] = wfrag(0); fr[1] = wfrag(1); fr[2] = wfrag(2);
    sfor<144>([&](auto S) {
        constexpr int s = S, k = s / 4, u = s % 4;
        if constexpr (s % 16 == 5) {
            constexpr int kend = k + 4 < 35 ? k + 4 : 35, issued = 20 + ((s + 1) / 2 < 52 ? (s + 1) / 2 : 52);
            waitv_bar<issued - 2 * kend - 2 + (s > 114 ? 3 : 0)>();
            sfor<kend - k>([&](auto J) { pinv(xf[k + 1 + J]); });
            if constexpr (s % 32 == 5) STAMPR(3 + s / 32);
        }
        if constexpr (s + 3 < 144) fr[(s + 3) % 4] = wfrag(s + 3);
        SBAR();
        const f32x16 cin = k == 0 ? (f32x16){} : acc[u];
        if constexpr (u < 3 && u >= 3 - NV) acc[u] = MFMA32(xf[k], fr[u], cin);
        else acc[u] = MFMA32(fr[u], xf[k], cin);
        SBAR();
        if constexpr (s < 104 && u == 0) wld(k + 10);
        if constexpr (s < 104 && u == 2) xld(std::integral_constant<int, (s < 104 ? k + 10 : 0)>{});
        if constexpr (s == 106 || s == 110) gst<0>(ws + O_OT + (size_t)(blockIdx.x * 9 + w + (s - 106)) * KB, l16, (u32x4){EMPTY, EMPTY, EMPTY, EMPTY});
        if constexpr (s == 114) asm volatile("global_store_dword %0, %1, %2" H_POL :: "v"(threadIdx.x * 4), "v"(EMPTY), "s"(ws + O_OT + (size_t)(blockIdx.x * 9 + 8) * KB) : "memory");
        if constexpr (s == 9) {
            PIN(sa); PIN(sb); stats_of(sa, sb, mu, rstd); PIN(mu); PIN(rstd);
            if constexpr (NV > 0) stl[lane] = h ? mu * rstd : rstd;
        }
        if constexpr (s == 11) {
            if (w < 2) {
                asm volatile("" : "+v"(cp[0]), "+v"(cp[1]), "+v"(cp[2]), "+v"(cp[3]), "+v"(cp[4]), "+v"(cp[5]), "+v"(cp[6]), "+v"(cp[7]), "+v"(cp[8]));
                csl[threadIdx.x] = (((cp[0] + cp[1]) + (cp[2] + cp[3])) + ((cp[4] + cp[5]) + (cp[6] + cp[7]))) + cp[8];
            }
        }
        SBAR();
    });
    STAMPR(8);
    unsigned pf = 0;
    PF_CODE(pf, "_Z5k_attILb1EEvPcPKtS2_S2_", 23 * KB);
    PF_CODEW(1, pf, "_Z5k_attILb0EEvPcPKtS2_S2_", 11 * KB);
    const float qscale = 0.23570226039551584f * 1.4426950408889634f;
    char* kvb = ws + O_KV + (tt >> 2) * KVG;
    sfor<3>([&](auto U) {
        constexpr int u = U;
        const int slot = 6 * cb + 2 * u, head = slot & 31;
        if constexpr (u >= 3 - NV) {
            const float cs = csl[u * 32 + c];
            u32x4 p[2];
#pragma unroll
            for (int m = 0; m < 4; ++m) {
                const f32x4 rs = *(const LDSP f32x4*)(stl + 8 * m + 4 * h), ms = *(const LDSP f32x4*)(stl + 32 + 8 * m + 4 * h);
                float v[4];
#pragma unroll
                for (int i = 0; i < 4; ++i) v[i] = acc[u][4 * m + i] * rs[i] - ms[i] * cs;
                p[m >> 1][2 * (m & 1)] = pk2(v[0], v[1]); p[m >> 1][2 * (m & 1) + 1] = pk2(v[2], v[3]);
            }
            char* d = kvb + (size_t)head * KVH + 4608 + (tt & 3) * 1280;
            const unsigned vo = (c >> 4) * KVH + h * 320;
            gst<0>(d, vo + swap23(c & 15) * 16, p[0]); gst<640>(d, vo + swap23(c & 15) * 16, p[1]);
            if ((c & 15) < 4) {
                const unsigned one = (c & 1) ? 0u : 0x3F803F80u;
                gst<288>(d, vo + (c & 1) * 16 + (c & 2) * 320, (u32x4){one, one, one, one});
            }
        } else {
            const bool isq = slot < 32;
            const float sc = isq ? rstd * qscale : rstd;
            char* d = isq ? ws + O_Q + (size_t)(head * 64 + tt) * KB : kvb + (size_t)head * KVH + (tt & 3) * KB;
#pragma unroll
            for (int a = 0; a < 2; ++a) {
                u32x4 p;
#pragma unroll
                for (int m = 0; m < 2; ++m) {
                    const f32x4 cs4 = *(const LDSP f32x4*)(csl + u * 32 + 16 * a + 8 * m + 4 * h);
                    float v[4];
#pragma unroll
                    for (int i = 0; i < 4; ++i) v[i] = (acc[u][8 * a + 4 * m + i] - mu * cs4[i]) * sc;
                    p[2 * m] = pk2(v[0], v[1]); p[2 * m + 1] = pk2(v[2], v[3]);
                }
                gst<0>(d + (isq ? 64 * KB : KVH) * a, l16, p);
            }
        }
    });
    {
        const f32x4 c0 = *(const LDSP f32x4*)(csl + 96 + 4 * h), c1 = *(const LDSP f32x4*)(csl + 104 + 4 * h);
        const float e[6] = {acc[3][0] - mu * c0[0], acc[3][1] - mu * c0[1], acc[3][2] - mu * c0[2], acc[3][3] - mu * c0[3],
                            acc[3][4] - mu * c1[0], acc[3][5] - mu * c1[1]};
#pragma unroll
        for (int m = 0; m < 3; ++m) {
            const int slot = 6 * cb + 3 * h + m, type = slot >> 5, head = slot & 31;
            const float sc = type == 0 ? rstd * qscale : rstd;
            const unsigned p = pk2(e[2 * m] * sc, e[2 * m + 1] * sc);
            const unsigned kvo = (unsigned)O_KV + head * KVH + (tt >> 2) * KVG;
            if (NV == 0 || type < 2) {
                const unsigned o = type == 0 ? (unsigned)O_QX + (head * 2048 + tt * 32 + c) * 4 : kvo + 4096 + (((tt >> 1) & 1) * 32 + c) * 8 + (tt & 1) * 4;
                asm volatile("global_store_dword %0, %1, %2" H_POL :: "v"(o), "v"(p), "s"(ws) : "memory");
            } else {
                const unsigned o = kvo + 4608 + (tt & 3) * 1280 + (c >> 4) * 640 + ((c >> 2) & 1) * 320 + 256 + ((c & 3) + 4 * ((c >> 3) & 1)) * 2;
                asm volatile("global_store_short %0, %1, %2" H_POL "\n\tglobal_store_short_d16_hi %0, %1, %2 offset:16" H_POL :: "v"(o), "v"(p), "s"(ws) : "memory");
            }
        }
    }
    PF_DONE(pf);
    STAMPR(9); STAMP_OUT(0, 10);
}
__global__ __launch_bounds__(256, 2) void k_qkv(char* ws) {
    extern __shared__ __attribute__((aligned(16))) char smem[];
    const int w = wave_id(), lane = threadIdx.x & 63;
    const int xcd = blockIdx.x & 7, r = blockIdx.x >> 3;
    const int rb = (xcd & 3) * 4 + (r & 3), cb = (xcd >> 2) * 8 + (r >> 2), tt = rb * 4 + w;
    if (cb < 10) qkv_body<0>(ws, smem, w, lane, tt, cb);
    else if (cb == 10) qkv_body<1>(ws, smem, w, lane, tt, cb);
    else qkv_body<3>(ws, smem, w, lane, tt, cb);
}

__device__ __forceinline__ bf16x8 packp(const f32x16& s, int b) {
    const u32x4 p = {pk2(s[b], s[b + 1]), pk2(s[b + 2], s[b + 3]), pk2(s[b + 4], s[b + 5]), pk2(s[b + 6], s[b + 7])};
    return __builtin_bit_cast(bf16x8, p);
}
__device__ __forceinline__ float halfmax(float m) {
    auto rr = __builtin_amdgcn_permlane32_swap(__float_as_uint(m), __float_as_uint(m), false, false);
    return fmaxf(__uint_as_float(rr[0]), __uint_as_float(rr[1]));
}
__device__ __forceinline__ float max16(const f32x16& s) {
    float m = s[0];
#pragma unroll
    for (int i = 1; i < 16; ++i) m = fmaxf(m, s[i]);
    return halfmax(m);
}

__device__ __forceinline__ void gld1(bf16x8& a, const char* sb, unsigned voff) {
    asm volatile("global_load_dwordx4 %0, %1, %2" : "=&v"(a) : "v"(voff), "s"(sb) : "memory");
}
__device__ __forceinline__ void gldw(unsigned& a, const char* sb, unsigned voff) {
    asm volatile("global_load_dword %0, %1, %2" : "=&v"(a) : "v"(voff), "s"(sb) : "memory");
}
__device__ __forceinline__ void dma1nt(const char* sb, unsigned voff, unsigned ld) {
    unsigned sv;
    asm volatile("s_mov_b32 %0, m0\n\ts_mov_b32 m0, %3\n\ts_nop 0\n\tglobal_load_lds_dwordx4 %1, %2 nt\n\ts_mov_b32 m0, %0"
                 : "=&s"(sv) : "v"(voff), "s"(sb), "s"(ld) : "memory");
}
__device__ __forceinline__ s16x4 vtr(lds_ptr p) { return __builtin_amdgcn_ds_read_tr16_b64_v4i16((LDSP s16x4*)p); }
constexpr int ATT_LDS = KVH + 2 * KB;
#define LSCHED 0x4444444324144044ull
#define SSCHED 0x4444432414404444ull
#define SIDE_N 9
#define BAL_G 11
#define FILL_G 3

template <bool SIDE>
__global__ __launch_bounds__(512, 2) void k_att(char* ws, const uint16_t* w1, const uint16_t* w3, const uint16_t* w2) {
    STAMP_DECL; STAMPR(0);
    extern __shared__ __attribute__((aligned(16))) char smem[];
    const int w = wave_id(), lane = threadIdx.x & 63, h = lane >> 5, c = lane & 31;
    const int head = 4 * (blockIdx.x & 7) + ((blockIdx.x >> 3) & 3), tt = (blockIdx.x >> 5) * 8 + w;
    const unsigned l16 = lane * 16, lds0 = (unsigned)(uintptr_t)smem;
    bf16x8 qf;
    unsigned qx;
    gld1(qf, ws + O_Q + (size_t)(head * 64 + tt) * KB, l16);
    gldw(qx, ws + O_QX + (size_t)(head * 2048 + tt * 32) * 4, c * 4);
    const char* kv = ws + O_KV + (size_t)head * KVH + w * KB;
#pragma unroll
    for (int i = 0; i < 4; ++i) dma1(kv + i * 8 * KB, l16, lds0 + w * KB + i * 8 * KB);
    STAMPR(5);
    const lds_ptr kp0 = (lds_ptr)smem + l16;
    const lds_ptr xp0 = (lds_ptr)smem + 4096 + lane * 8;
    const lds_ptr vp0 = (lds_ptr)smem + 4608 + h * 320 + (c < 19 ? c : 19) * 16;
    WAITV_BAR(0);
    PIN(qf); PIN(qx); STAMPR(1);
    s16x4 qv[4];
#pragma unroll
    for (int t = 0; t < 4; ++t) {
        const unsigned z = (h == (t >> 1)) ? qx : 0u;
        qv[t] = __builtin_bit_cast(s16x4, (t & 1) ? (u32x2){0u, z} : (u32x2){z, 0u});
    }
    f32x16 o = {}, cm, sA, sB;
    s16x4 kx = *(LDSP s16x4*)xp0, kxn;
    sA = MFMA32K8(kx, qv[0], o);
    sA = MFMA32(ldsfrag(kp0), qf, sA);
    bf16x8 kA = ldsfrag(kp0 + 1024), kB;
    {
        const float m0 = max16(sA);
#pragma unroll
        for (int i = 0; i < 16; ++i) { cm[i] = -m0; sA[i] -= m0; }
    }
    u32x4 pl, ph = {};
    bf16x8 vl, vh = {};
    sB = o;
    PIN(cm); PIN(ph); PIN(vh); PIN(sB);
#define EXP4(X, i) do { X[i] = __builtin_amdgcn_exp2f(X[i]); X[i + 1] = __builtin_amdgcn_exp2f(X[i + 1]); \
        X[i + 2] = __builtin_amdgcn_exp2f(X[i + 2]); X[i + 3] = __builtin_amdgcn_exp2f(X[i + 3]); PIN(X); } while (0)
#define ATT_STEP(SC, SN, KX, QV, KU, KL, KOFF, VOFF, EXTRA) do { \
        KL = ldsfrag(kp + (KOFF)); SBAR(); \
        ph[2] = pk2(SN[12], SN[13]); ph[3] = pk2(SN[14], SN[15]); PIN(ph); SBAR(); \
        SN = MFMA32K8(KX, QV, cm); SBAR(); EXP4(SC, 0); SBAR(); \
        vl = ldsfrag(vp + (VOFF)); EXTRA; SBAR(); \
        o = MFMA32(vh, __builtin_bit_cast(bf16x8, ph), o); SBAR(); \
        pl[0] = pk2(SC[0], SC[1]); pl[1] = pk2(SC[2], SC[3]); PIN(pl); SBAR(); EXP4(SC, 4); SBAR(); \
        vh = ldsfrag(vp + (VOFF) + 640); SBAR(); \
        SN = MFMA32(KU, qf, SN); SBAR(); \
        pl[2] = pk2(SC[4], SC[5]); pl[3] = pk2(SC[6], SC[7]); PIN(pl); SBAR(); EXP4(SC, 8); SBAR(); \
        o = MFMA32(vl, __builtin_bit_cast(bf16x8, pl), o); SBAR(); \
        ph[0] = pk2(SC[8], SC[9]); ph[1] = pk2(SC[10], SC[11]); PIN(ph); SBAR(); EXP4(SC, 12); SBAR(); } while (0)
    const FfnJob J0 = ffn_job(blockIdx.x * 8 + w), J1 = ffn_job(blockIdx.x * 8 + w + 4);
    const int jm = J0.m, jld = jm < 2 ? F_ * 2 : D_ * 2;
    const unsigned joff = (lane >> 3) * jld + (lane & 7) * 16;
    s16x4 ja, jb;
    float jc[4];
    const int jp = jm < 2 ? ((lane & 1) << 1) | ((lane >> 1) & 1) : lane & 3;
    const lds_ptr jt = (lds_ptr)smem + w * 4 * KB + (lane >> 4) * KB + ((lane & 15) >> 2) * 128 + jp * 8;
    const unsigned jo = jm < 2 ? (lane >> 4) * 512 + jm * 256 + (lane & 15) * 16 : l16;
    const int c1 = jm < 2 ? 36 : 1, c2 = jm < 2 ? 0 : 1287, c3 = jm < 2 ? 2 : 9;
    const char* jsrc = (const char*)(jm == 0 ? w1 : jm == 1 ? w3 : w2);
    char* jdst = ws + (jm < 2 ? O_W13 : O_W2);
    const int t2 = (blockIdx.x >> 5) * 8 + (head >> 2), db = head & 3;
    const size_t xb = (size_t)(t2 * 36 + 9 * db) * KB;
    const char* wb = ws + O_WO + (size_t)(9 * db) * 18 * KB;
    const unsigned xo8 = (h * 32 + (w & 1) * 16 + (lane & 15)) * 16 + (lane & 16) / 2;
    bf16x8 wf[18], xr;
    u32x2 x8;
    auto side = [&](auto I) {
        constexpr int i = I, n = (i & 7) / 2, lu = (i >> 6) & 7, su = (i >> 9) & 7;
        if constexpr (i < 19) dma1(kv + i * 8 * KB, l16, lds0 + w * KB + i * 8 * KB);
        if constexpr (i >= 32 && su < 4 && i % 2 == 0) {
            const int cg = (su < 2 ? J0.cbk : J1.cbk) * 4 + n;
            const u32x2 a = __builtin_bit_cast(u32x2, ja), b = __builtin_bit_cast(u32x2, jb);
            const u32x4 p = {a[0], a[1], b[0], b[1]};
            gst<0>(jdst + (size_t)(cg * c1 + cg / 9 * c2 + ((su < 2 ? J0.kb : J1.kb) * 2 + (su & 1)) * c3) * KB, jo, p);
            jc[n] = sum8(p, (su & 1) ? jc[n] : 0.f); pinv(jc[n]);
            if constexpr (n < 3) { const lds_ptr q = jt + (su < 2 ? 0 : su - 1) * 16 * KB + (n + 1) * 32; ja = vtr(q); jb = vtr(q + 512); }
        }
        if constexpr (i >= 4096) {
            constexpr int m = (i >> 12) * 8 - 8 + (i & 7);
            if constexpr (m < 18) gldo<(m & 3) * KB>(wf[m], wb + (18 * w + (m & ~3)) * KB, l16);
            if constexpr (m == 18 || m == 19) dma1(wb + (8 * 18 + 8 * (m - 18) + w) * KB, l16, lds0 + (48 + 8 * (m - 18) + w) * KB);
            if constexpr (m == 20) dma1(wb + (8 * 18 + 16 + (w & 1)) * KB, l16, lds0 + (48 + 16 + (w & 1)) * KB);
            if constexpr (m == 21) gld1(xr, ws + O_XT + xb + w * KB, l16);
            if constexpr (m == 22) asm volatile("global_load_dwordx2 %0, %1, %2" : "=&v"(x8) : "v"(xo8), "s"(ws + O_XT + xb + 8 * KB) : "memory");
        }
        if constexpr (i >= 32 && lu < 4 && i % 2 == 1)
            dma1nt(jsrc + (size_t)((lu < 2 ? J0.kb : J1.kb) * 64 + (lu & 1) * 32 + n * 8) * jld + (lu < 2 ? J0.cbk : J1.cbk) * 128, joff,
                   lds0 + ((lu < 2 ? 0 : lu - 1) * 16 + w * 4 + n) * KB);
    };
#define KVD(i) side(IC(i))
#define ATT_GROUP(g, P) do { \
        const lds_ptr kp = kp0 + (g) * KVG, vp = vp0 + (g) * KVG, xp = xp0 + ((g) < 15 ? (g) + 1 : (g)) * KVG; \
        ATT_STEP(sA, sB, kx, qv[1], kA, kB, 2048, 0, KVD(P); KVD(P + 1)); \
        ATT_STEP(sB, sA, kx, qv[2], kB, kA, 3072, 1280, kxn = *(LDSP s16x4*)xp; KVD(P + 2); KVD(P + 3)); \
        ATT_STEP(sA, sB, kx, qv[3], kA, kB, KVG, 2560, KVD(P + 4); KVD(P + 5));     \
        ATT_STEP(sB, sA, kxn, qv[0], kB, kA, KVG + 1024, 3840, KVD(P + 6); KVD(P + 7)); \
        kx = kxn; } while (0)
    ATT_GROUP(0, 4);
    ATT_GROUP(1, 12);
    WAITV_BAR(10);
    auto fill = [&] {
        if (w >= 4) return;
        char* pb = ws + O_PART + (size_t)blockIdx.x * 27 * KB + w * KB;
        const u32x4 e = {EMPTY, EMPTY, EMPTY, EMPTY};
#pragma unroll
        for (int i = 0; i < 7; ++i) if (i < 6 || w < 3) gst<0>(pb + i * 4 * KB, l16, e);
    };
    constexpr int PLAIN = 32 + 4 * 64 + 4 * 512;
    auto prio = [&](int g) {
        if (w >= 4) { if (g & 1) __builtin_amdgcn_s_setprio(1); else __builtin_amdgcn_s_setprio(0); }
    };
    int g0 = 2;
    if (SIDE && w < 4) {
        sfor<SIDE_N>([&](auto G) {
            constexpr int g = 2 + G, lu = (LSCHED >> (4 * g)) & 15, su = (SSCHED >> (4 * g)) & 15;
            if constexpr (g == 4) { STAMPR(12); } if constexpr (g == 5) { STAMPR(13); } if constexpr (g == 9) { STAMPR(14); }
            if constexpr (g == 7) { STAMPR(10); waitvl_bar<0>(); STAMPR(11); }
            if constexpr (g == FILL_G) fill();
            if constexpr (su < 4) { waitv<4>(); const lds_ptr q = jt + (su < 2 ? 0 : su - 1) * 16 * KB; ja = vtr(q); jb = vtr(q + 512); }
            ATT_GROUP(g, 32 + lu * 64 + su * 512);
            if constexpr (su & 1) {
                const auto x = __builtin_amdgcn_permlane32_swap(__float_as_uint(jc[0]), __float_as_uint(jc[2]), false, false);
                const auto y = __builtin_amdgcn_permlane32_swap(__float_as_uint(jc[1]), __float_as_uint(jc[3]), false, false);
                const auto z = __builtin_amdgcn_permlane16_swap(__float_as_uint(__uint_as_float(x[0]) + __uint_as_float(x[1])),
                                                                __float_as_uint(__uint_as_float(y[0]) + __uint_as_float(y[1])), false, false);
                const FfnJob J = su < 2 ? J0 : J1;
                asm volatile("global_store_dword %0, %1, %2" H_POL :: "v"(((lane & 48) + swap23(lane & 15)) * 4), "v"(__uint_as_float(z[0]) + __uint_as_float(z[1])),
                             "s"(ws + O_CS + (jm < 2 ? J.kb * NCS + jm * F_ + J.cbk * 64 : 9 * NCS + blockIdx.x * 64) * 4) : "memory");
                SBAR();
            }
        });
        g0 = 2 + SIDE_N;
    }
    for (int g = g0; g < 12; ++g) {
        if (g == 7) { STAMPR(10); waitvl_bar<0>(); STAMPR(11); }
        if (g == 4) { STAMPR(12); } if (g == 5) { STAMPR(13); } if (g == 9) { STAMPR(14); } if (g == 11) { STAMPR(15); }
        if (!SIDE && g == FILL_G) fill();
        if (!SIDE || g >= BAL_G) prio(g);
        ATT_GROUP(g, PLAIN);
    }
    prio(0); ATT_GROUP(12, PLAIN + 4096);
    prio(1); ATT_GROUP(13, PLAIN + 2 * 4096);
    prio(0); ATT_GROUP(14, PLAIN + 3 * 4096);
    prio(1); ATT_GROUP(15, PLAIN);
    waitv<0>();
    sfor<18>([&](auto M) { pinv(wf[M]); });
    pinv(xr); pinv(x8);
    ph[2] = pk2(sB[12], sB[13]); ph[3] = pk2(sB[14], sB[15]);
    o = MFMA32(vh, __builtin_bit_cast(bf16x8, ph), o);
    STAMPR(2);
    const bool bad = !(o[10] < 1e30f) | !(fabsf((((o[0] + o[1]) + (o[2] + o[3])) + ((o[4] + o[5]) + (o[6] + o[7]))) + (o[8] + o[9])) < 3e38f);
    if (__builtin_expect(__any(bad), 0)) {
        float m = -INFINITY;
        o = (f32x16){};
        const f32x16 z = {};
        const char* kg = ws + O_KV + (size_t)head * KVH;
        for (int t = 0; t < 64; ++t) {
            const int tl = t & 3;
            const char* gp = kg + (t >> 2) * KVG;
            const char* gv = gp + 4608 + tl * 1280 + h * 320 + (c < 19 ? c : 19) * 16;
            const unsigned zq = (h == (tl >> 1)) ? qx : 0u;
            const s16x4 qq = __builtin_bit_cast(s16x4, (tl & 1) ? (u32x2){0u, zq} : (u32x2){zq, 0u});
            f32x16 s = MFMA32K8(*(const s16x4*)(gp + 4096 + lane * 8), qq, z);
            s = MFMA32(*(const bf16x8*)(gp + tl * 1024 + l16), qf, s);
            const float mn = fmaxf(m, max16(s)), al = __builtin_amdgcn_exp2f(m - mn);
            m = mn;
#pragma unroll
            for (int i = 0; i < 16; ++i) { s[i] = __builtin_amdgcn_exp2f(s[i] - mn); o[i] *= al; }
            o = MFMA32(*(const bf16x8*)gv, packp(s, 0), o);
            o = MFMA32(*(const bf16x8*)(gv + 640), packp(s, 8), o);
        }
    }
    const float rl = __builtin_amdgcn_rcpf(__uint_as_float(__builtin_amdgcn_permlane32_swap(__float_as_uint(o[10]), __float_as_uint(o[10]), false, false)[0]));
    char* ot = ws + O_OT + (size_t)tt * 36 * KB;
    const unsigned oo = (c >> 4) * 18 * KB + (c & 15) * 16;
    const u32x4 p = {pk2s(o[0] * rl, o[1] * rl), pk2s(o[2] * rl, o[3] * rl), pk2s(o[4] * rl, o[5] * rl), pk2s(o[6] * rl, o[7] * rl)};
    gst<0>(ot + (head >> 1) * KB + (head & 1) * 512, oo + h * 256, p);
    if (h == 0) asm volatile("global_store_dword %0, %1, %2" H_POL :: "v"(oo), "v"(pk2s(o[8] * rl, o[9] * rl)),
                             "s"(ot + (16 + (head >> 4)) * KB + ((head >> 2) & 3) * 256 + (head & 3) * 4) : "memory");
    unsigned pf = 0;
    PF_CODE(pf, "_Z5k_ff1Pc", 17 * KB);
    const lds_ptr st = (lds_ptr)smem + 72 * KB + l16;
    LDSP float* red = (LDSP float*)((lds_ptr)smem + 108 * KB);
    f32x4 acc[2];
    { const u32x4 x = swap_pair(__builtin_bit_cast(u32x4, xr)); acc[0] = unpk(x[0], x[1]); acc[1] = unpk(x[2], x[3]); }
    STAMPR(8); STAMPR(9);
    waitvl_bar<0>();
    STAMPR(3);
    {
        const char* src = ws + O_OT + (size_t)(t2 * 36 + w) * KB;
        u32x4 f[5];
#pragma nounroll
        for (int spin = 0; spin < SPIN_CAP; ++spin) {
            asm volatile("global_load_dwordx4 %0, %5, %6 sc1\n\tglobal_load_dwordx4 %1, %5, %7 sc1\n\tglobal_load_dwordx4 %2, %5, %8 sc1\n\t"
                         "global_load_dwordx4 %3, %5, %9 sc1\n\tglobal_load_dwordx4 %4, %10, %11 sc1\n\ts_waitcnt vmcnt(0)"
                         : "=&v"(f[0]), "=&v"(f[1]), "=&v"(f[2]), "=&v"(f[3]), "=&v"(f[4])
                         : "v"(l16), "s"(src), "s"(src + 8 * KB), "s"(src + 16 * KB), "s"(src + 24 * KB), "v"(c * 16), "s"(src + 32 * KB - w * 512) : "memory");
            const unsigned m = max(max(max(umax4(f[0]), umax4(f[1])), max(umax4(f[2]), umax4(f[3]))), umax4(f[4]));
            if (!__any(m == EMPTY)) break;
            __builtin_amdgcn_s_sleep(1);
        }
        STAMPR(4);
#pragma unroll
        for (int i = 0; i < 4; ++i) *(LDSP u32x4*)(st + (w + 8 * i) * KB) = f[i];
        if (h == 0) *(LDSP u32x4*)(st + 32 * KB + w * 512) = f[4];
    }
    LDS_BAR();
    STAMPR(6);
#pragma unroll
    for (int k = 0; k < 18; ++k) {
        acc[0] = MFMA16(wf[k], ldsfrag(st + k * KB), acc[0]);
        acc[1] = MFMA16(wf[k], ldsfrag(st + (18 + k) * KB), acc[1]);
    }
    {
        const u32x4 y = swap_pair((u32x4){pk2(acc[0][0], acc[0][1]), pk2(acc[0][2], acc[0][3]), pk2(acc[1][0], acc[1][1]), pk2(acc[1][2], acc[1][3])});
        gst<0>(ws + O_X1T + xb + w * KB, l16, y);
        float a = 0.f, b = 0.f;
#pragma unroll
        for (int i = 0; i < 4; ++i) stat_add(y[i], a, b);
        const auto t = __builtin_amdgcn_permlane32_swap(__float_as_uint(a), __float_as_uint(b), false, false);
        red[w * 64 + c * 2 + h] = __uint_as_float(t[0]) + __uint_as_float(t[1]);
    }
    if (w < 2) {
        acc[0] = unpk(x8[0], x8[1]); acc[1] = (f32x4){};
#pragma unroll
        for (int k = 0; k < 18; ++k)
            acc[k & 1] = MFMA16(ldsfrag((lds_ptr)smem + l16 + (48 + k) * KB), ldsfrag(st + (18 * w + k) * KB), acc[k & 1]);
        acc[0] += acc[1];
        const u32x2 y = {pk2(acc[0][0], acc[0][1]), pk2(acc[0][2], acc[0][3])};
        *(u32x2*)(ws + O_X1T + xb + 8 * KB + xo8) = y;
        float a = 0.f, b = 0.f;
        stat_add(y[0], a, b); stat_add(y[1], a, b);
        const float r = stat_red(a, b);
        if (!(lane & 16)) red[512 + (16 * w + (lane & 15)) * 2 + h] = r;
    }
    LDS_BAR();
    if (w == 7) {
        float t = red[512 + lane];
#pragma unroll
        for (int i = 0; i < 8; i += 2) t += red[i * 64 + lane] + red[i * 64 + 64 + lane];
        ((float*)(ws + O_STB))[(32 * t2 + (lane >> 1)) * 8 + db * 2 + (lane & 1)] = t;
    }
    PF_DONE(pf);
    STAMPR(7); STAMP_OUT(SIDE ? 6 : 1, 16);
}

constexpr int SLOT = 36 * KB;
constexpr int FF_LDS = 4 * SLOT + 288 * 4;
namespace f1 {
constexpr int NS = 324, PF = 2;
constexpr int tile(int s) { return s < 84 ? s % 3 : s < 108 ? (s - 84) / 8 : 3 + (s - 108) / 36; }
constexpr int kst(int s) { return s < 84 ? s / 3 : s < 108 ? 28 + (s - 84) % 8 : (s - 108) % 36; }
constexpr int piece(int t, int k) {
    return t < 4 ? 36 * t + k : (t == 4 || t == 8) ? (k < 20 ? k : k + 16) : t == 5 ? 72 + k : t == 6 ? (k < 16 ? k + 20 : k + 36) : 108 + k;
}
constexpr int ES[8] = {93, 110, 134, 158, 182, 218, 254, 290}, ED[8] = {2, 3, 3, 3, 4, 4, 4, 4};
constexpr int TI[9] = {0, 0, 0, 74, 86, 108, 128, 182, 218};
constexpr int tstep(int t) { return t == 3 ? 2 : 4; }
constexpr int tlast(int t) { return TI[t] + 4 * tstep(t); }
constexpr int item(int i) {
    return i < 4 ? 8 + i : i < 7 ? -1 - (i - 4) : i < 15 ? 5 + i : i < 18 ? -4 - (i - 15) : i < 26 ? 2 + i : i < 29 ? -7 - (i - 26) : i - 1;
}
constexpr int xslot(int k) { for (int i = 0; i < 37; ++i) if (item(i) == k) return 2 * i; return -1; }
constexpr int vops(int s) {
    int n = (s % 2 == 0 && s < 74) ? 1 : 0;
    for (int t = 3; t < 9; ++t) if (s >= TI[t] && s < tlast(t) && (s - TI[t]) % tstep(t) == 0) n += 1;
    for (int e = 0; e < 8; ++e) if (s == ES[e] + 7 * ED[e] + 4) n += 1;
    return n;
}
constexpr int between(int a, int b) { int n = 0; for (int s = a + 1; s < b; ++s) n += vops(s); return n; }
}
#define FF1_M16 1
__global__ __launch_bounds__(512, 2) void k_ff1(char* ws) {
    using namespace f1;
    STAMP_DECL; STAMPR(0);
    extern __shared__ __attribute__((aligned(16))) char smem[];
    const int w = wave_id(), lane = threadIdx.x & 63;
    const int xcd = blockIdx.x & 7, r = blockIdx.x >> 3;
    const int rb = (xcd & 1) * 4 + (r & 3), cbk = (xcd >> 1) * 8 + (r >> 2), tt = rb * 8 + w, ft0 = cbk * 9;
    const unsigned l16 = lane * 16, lds0 = (unsigned)(uintptr_t)smem;
    const char* wsrc = ws + O_W13 + (size_t)ft0 * SLOT;
    const char* xsrc = ws + O_X1T + (size_t)tt * 36 * KB;
    const int g = lane >> 4, c = lane & 15;
    const unsigned fo = g * 512 + c * 16;
    constexpr int NU = 2;
    bf16x8 xf[36];
    f32x4 sa[NU], sb[NU];
    float cp[9];
    const int ci = threadIdx.x < 224 ? 0 : threadIdx.x - 224;
    auto wpiece = [&](int t, int k) { dma1(wsrc + t * SLOT + k * KB, l16, lds0 + t * SLOT + k * KB); };
    auto xld = [&](auto K) { constexpr int k = K;
        gldo<FF1_M16 ? (k & 2) * KB + (k & 1) * 256 : (k & 3) * KB>(xf[k], xsrc + (k & ~3) * KB, fo); };
    if (w < 6) { wpiece(w >> 1, (w & 1) * 2); wpiece(w >> 1, (w & 1) * 2 + 1); }
    xld(IC(0)); xld(IC(1)); xld(IC(2)); xld(IC(3));
    asm volatile("s_barrier" ::: "memory");
#pragma unroll
    for (int u = 0; u < NU; ++u) gld_stats(sa[u], sb[u], ws + O_STB + tt * 1024 + u * 512, c * 32);
    if (w >= 3) {
        const int t = ci >> 5, rho = ci & 31;
        const unsigned o = ((rho >> 4) * F_ + (ft0 + t) * 16 + swap23(rho & 15)) * 4;
#pragma unroll
        for (int i = 0; i < 9; ++i) gldf(cp[i], ws + O_CS + i * NCS * 4, o);
    }
    wpiece(0, 4 + w); wpiece(1, 4 + w); wpiece(2, 4 + w); xld(IC(4)); xld(IC(5)); xld(IC(6)); xld(IC(7));
    unsigned lb[3] = {lds0 + fo, lds0 + fo + 64 * KB, lds0 + fo + 128 * KB};
    unsigned csb = lds0 + 4 * SLOT + 16 * g, hoff = (g >> 1) * 144 * KB + c * 16 + (g & 1) * 256;
    auto wfrag = [&](int t, int k) { const int p = piece(t, k & ~1); return *(const LDSP bf16x8*)(uintptr_t)(lb[p >> 6] + (p & 63) * KB + (k & 1) * 256); };
    f32x4 acc[3][4];
    PIN(lb[0]); PIN(lb[1]); PIN(lb[2]); PIN(csb); PIN(hoff);
    const LDSP float* csr = (const LDSP float*)(uintptr_t)csb;
    char* hdst = ws + O_HT + (size_t)tt * 288 * KB + (size_t)ft0 * 512 + 2048;
    bf16x8 fr[3];
    f32x4 cg, cu;
    u32x4 hp;
    float mu[NU], nrl[NU], r2[NU], em[2], ep[2], hv;
#define PIN4(b) asm volatile("" : "+v"(xf[b]), "+v"(xf[b + 1]), "+v"(xf[b + 2]), "+v"(xf[b + 3]))
    STAMPR(1);
    waitv_bar<7 + 2 * NU>(); PIN4(0);
    STAMPR(2);
    fr[0] = wfrag(tile(0), kst(0)); fr[1] = wfrag(tile(1), kst(1));
    auto epi = [&](auto E_, auto R_, auto P_) {
        constexpr int e = E_, rel = R_, d = e < 8 ? ED[e < 8 ? e : 0] : 2, part = P_;
        if constexpr (part == 0) {
            if constexpr (rel >= 3 && (rel - 3) % d == 0 && (rel - 3) / d < 8) {
                constexpr int v = (rel - 3) / d;
                if constexpr (v & 1) { hp[v >> 1] = pk2(hv, ep[1] * em[1]); PIN(hp); } else { hv = ep[0] * em[0]; PIN(hv); }
            }
            if constexpr (rel >= 2 && (rel - 2) % d == 0 && (rel - 2) / d < 8) {
                constexpr int v = (rel - 2) / d;
                constexpr int u = FF1_M16 ? v >> 2 : 0;
                em[v & 1] = __builtin_amdgcn_rcpf(__builtin_fmaf(em[v & 1], r2[u], r2[u])); PIN(em[v & 1]);
            }
        } else {
            if constexpr (rel == -2) { cg = *(const LDSP f32x4*)(csr + e * 32); cu = *(const LDSP f32x4*)(csr + e * 32 + 16); }
            if constexpr (rel >= 1 && (rel - 1) % d == 0 && (rel - 1) / d < 8) {
                constexpr int v = (rel - 1) / d;
                em[v & 1] = __builtin_amdgcn_exp2f(em[v & 1]); PIN(em[v & 1]);
            }
            if constexpr (rel >= 0 && rel % d == 0 && rel / d < 8) {
                constexpr int v = rel / d, u = FF1_M16 ? v >> 2 : 0;
                float tg = acc[e % 3][u][v & 3] - mu[u] * cg[v & 3]; PIN(tg);
                float tu = acc[e % 3][2 + u][v & 3] - mu[u] * cu[v & 3]; PIN(tu);
                em[v & 1] = tg * nrl[u]; PIN(em[v & 1]); ep[v & 1] = tg * tu; PIN(ep[v & 1]);
                if constexpr (v == 3 && !FF1_M16) { SBAR(); cg = *(const LDSP f32x4*)(csr + e * 32 + 8); cu = *(const LDSP f32x4*)(csr + e * 32 + 24); }
            }
            if constexpr (rel == 7 * d + 4) {
                const auto a = __builtin_amdgcn_permlane32_swap(hp[0], hp[2], false, false), b = __builtin_amdgcn_permlane32_swap(hp[1], hp[3], false, false);
                gst<e * 512 - 2048>(hdst, hoff, (u32x4){a[0], b[0], a[1], b[1]});
            }
        }
    };
    sfor<NS>([&](auto S) {
        constexpr int s = S, t = tile(s), k = kst(s);
        if constexpr (s == 10) { waitv_bar<between(-1, s)>(); PIN4(4); STAMPR(3); }
        if constexpr (s == 24 || s == 48 || s == 72) { waitv<between(xslot(s / 3 + 3), s)>(); PIN4(s / 3); }
        if constexpr (s == 34 || s == 58 || s == 82) { waitv_bar<between(xslot((s + 2) / 3 + 3), s)>(); PIN4((s + 2) / 3); STAMPR(4 + (s - 34) / 24); }
        if constexpr (s == 88) { waitv<between(xslot(35), s)>(); PIN4(32); }
        if constexpr (s == 106) { waitvl_bar<between(tlast(3), s)>(); STAMPR(7); }
        if constexpr (s > 108 && s < 300 && (s - 106) % 36 == 0) { waitv_bar<between(tlast(3 + (s - 106) / 36), s)>(); STAMPR(7 + (s - 106) / 36); }
        if constexpr (s + PF < NS) fr[(s + PF) % 3] = wfrag(tile(s + PF), kst(s + PF));
        SBAR();
        if constexpr (k < 2) mf16z(acc[t % 3][2 * k], fr[s % 3], xf[0]);
        else mf16(acc[t % 3][2 * (k & 1)], fr[s % 3], xf[k & ~1]);
        SBAR();
        sfor<8>([&](auto E) { constexpr int rel = s - ES[E]; if constexpr (rel >= -2 && rel <= 7 * ED[E] + 4) epi(E, std::integral_constant<int, rel>{}, IC(0)); });
        SBAR();
        if constexpr (k < 2) mf16z(acc[t % 3][2 * k + 1], fr[s % 3], xf[1]);
        else mf16(acc[t % 3][2 * (k & 1) + 1], fr[s % 3], xf[k | 1]);
        SBAR();
        if constexpr (s % 2 == 0 && s < 74) {
            constexpr int it = item(s / 2);
            if constexpr (it >= 0) xld(std::integral_constant<int, it >= 0 ? it : 0>{});
            else wpiece((-1 - it) % 3, 12 + 8 * ((-1 - it) / 3) + w);
        }
        if constexpr (s == 13) {
#pragma unroll
            for (int u = 0; u < NU; ++u) {
                float rstd;
                PIN(sa[u]); PIN(sb[u]); stats_of(sa[u], sb[u], mu[u], rstd);
                nrl[u] = -rstd * 1.4426950408889634f; r2[u] = 1.0f / (rstd * rstd); PIN(mu[u]); PIN(nrl[u]); PIN(r2[u]);
            }
        }
        if constexpr (s == 15) {
            if (w >= 3) {
                asm volatile("" : "+v"(cp[0]), "+v"(cp[1]), "+v"(cp[2]), "+v"(cp[3]), "+v"(cp[4]), "+v"(cp[5]), "+v"(cp[6]), "+v"(cp[7]), "+v"(cp[8]));
                ((LDSP float*)((lds_ptr)smem + 4 * SLOT))[ci] = (((cp[0] + cp[1]) + (cp[2] + cp[3])) + ((cp[4] + cp[5]) + (cp[6] + cp[7]))) + cp[8];
            }
        }
        sfor<6>([&](auto T) {
            constexpr int tl = T + 3, j = (s - TI[tl]) / tstep(tl);
            if constexpr (s >= TI[tl] && s <= tlast(tl) && (s - TI[tl]) % tstep(tl) == 0) {
                if constexpr (j < 4) {
                    const int k4 = 4 * w;
                    dma1(wsrc + tl * SLOT + (k4 + j) * KB, l16, lds0 + (k4 + j + (k4 < 16 ? piece(tl, 0) : k4 < 20 ? piece(tl, 16) - 16 : piece(tl, 20) - 20)) * KB);
                } else if (w < 4) dma1(wsrc + tl * SLOT + (32 + w) * KB, l16, lds0 + (piece(tl, 32) + w) * KB);
            }
        });
        sfor<8>([&](auto E) { constexpr int rel = s - ES[E]; if constexpr (rel >= -2 && rel <= 7 * ED[E] + 4) epi(E, std::integral_constant<int, rel>{}, IC(1)); });
        if constexpr (s == NS - 2) epi(IC(8), IC(-2), IC(1));
        SBAR();
    });
    STAMPR(13);
    unsigned pf = 0;
    PF_CODE(pf, "_Z5k_ff2PcPt", 10 * KB);
    asm volatile("s_nop 15" ::: "memory"); SBAR();
    sfor<19>([&](auto R) { epi(IC(8), R, IC(0)); epi(IC(8), R, IC(1)); });
    PF_DONE(pf);
    STAMPR(14); STAMP_OUT(3, 15);
}

template <int OFF> __device__ __forceinline__ void gst2(char* sb, unsigned voff, u32x2 v) {
    asm volatile("global_store_dwordx2 %0, %1, %2 offset:%3" H_POL :: "v"(voff), "v"(v), "s"(sb), "n"(OFF) : "memory");
}
__global__ __launch_bounds__(512, 2) void k_ff2(char* ws, uint16_t* out) {
    STAMP_DECL; STAMPR(0);
    extern __shared__ __attribute__((aligned(16))) char smem[];
    const int w = wave_id(), lane = threadIdx.x & 63;
    const int xcd = blockIdx.x & 7, r = blockIdx.x >> 3;
    const int rb = (xcd & 1) * 8 + (r >> 2), db = r & 3, q = xcd >> 1, tg = w & 3, kh = w >> 2;
    const unsigned l16 = lane * 16, lds0 = (unsigned)(uintptr_t)smem;
    const char* wsrc = ws + O_W2 + (size_t)(db * 144 + 36 * q) * 9 * KB;
    const int t16 = rb * 8 + tg * 2;
    const char* h0 = ws + O_HT + ((size_t)t16 * 144 + 36 * q + 2 * kh) * KB;
    bf16x8 hf[3][4];
    auto item = [&](auto C, auto M) {
        constexpr int cn = C, m = M;
        if constexpr (m < 4) dma1(wsrc + cn * SLOT + (4 * w + m) * KB, l16, lds0 + (cn & 3) * SLOT + (4 * w + m) * KB);
        else if constexpr (m == 4) { if (w < 4) dma1(wsrc + cn * SLOT + (32 + w) * KB, l16, lds0 + (cn & 3) * SLOT + (32 + w) * KB); }
        else if constexpr (cn == 8) gldo<(m - 5) * KB>(hf[2][m - 5], h0 + (kh * 142 + 32) * KB, l16);
        else gldo<((m - 5) >> 1) * KB>(hf[cn % 3][((m - 5) & 1) * 2 + ((m - 5) >> 1)], h0 + (((m - 5) & 1) ^ kh) * 144 * KB + cn * 4 * KB, l16);
    };
    sfor<9>([&](auto M) { item(IC(0), M); });
    asm volatile("s_barrier" ::: "memory");
    sfor<9>([&](auto M) { item(IC(1), M); });
    unsigned lb[3] = {lds0 + l16 + kh * 18 * KB, lds0 + l16 + (kh * 18 + 64) * KB, lds0 + l16 + (kh * 18 + 128) * KB};
    PIN(lb[0]); PIN(lb[1]); PIN(lb[2]);
    auto ldt = [](int t) { return t < 4 ? 8 : (t - 4) / 8 * 2 + (t & 1); };
    auto lkk = [](int t) { return t < 4 ? t : ((t - 4) & 7) >> 1; };
    auto wfrag = [&](int s) {
        if (s >= 144) return ldsfrag((lds_ptr)smem + l16 + (lkk(s - 144) * 9 + ldt(s - 144)) * KB);
        const int p = ((s / 18) & 3) * 36 + s % 18;
        return *(const LDSP bf16x8*)(uintptr_t)(lb[p >> 6] + (p & 63) * KB);
    };
    f32x4 acc[2][9];
    bf16x8 fr[6], xr;
    u32x2 x8;
    const int g = lane >> 4, tt = rb * 4 + q;
    const unsigned xo8 = ((g >> 1) * 32 + 16 * kh + (lane & 15)) * 16 + (g & 1) * 8, xo = (xo8 & ~8u) + (g & 1) * KB;
#define PINH(c) asm volatile("" : "+v"(hf[(c) % 3][0]), "+v"(hf[(c) % 3][1]), "+v"(hf[(c) % 3][2]), "+v"(hf[(c) % 3][3]))
    STAMPR(1);
    waitv_bar<8>(); PINH(0);
    STAMPR(2);
    fr[0] = wfrag(0); fr[1] = wfrag(1);
    LDSP f32x4* xw = (LDSP f32x4*)((lds_ptr)smem + SLOT + w * 9 * KB + l16);
    const LDSP f32x4* xi = (const LDSP f32x4*)((lds_ptr)smem + SLOT + (w ^ 4) * 9 * KB + l16);
    LDSP f32x4* yw = (LDSP f32x4*)((lds_ptr)smem + 3 * SLOT + kh * 8 * KB + l16);
    char* dst = ws + O_PART + (size_t)(((t16 + kh) * 4 + db) * 3 + ((q - tg - 1) & 3)) * 4608;
    f32x4 v[3];
    auto send = [&](auto P) {
        constexpr int p = P;
        if constexpr (p == 4) {
            v[2] = acc[0][8] + acc[1][8];
            if (tg != q) gst2<0>(dst + 4096, lane * 8, (u32x2){pk2s(v[2][0], v[2][1]), pk2s(v[2][2], v[2][3])});
        } else {
            const f32x4 a = acc[0][2 * p] + acc[1][2 * p], b = acc[0][2 * p + 1] + acc[1][2 * p + 1];
            if (tg != q) gst<p * KB>(dst, l16, (u32x4){pk2s(a[0], a[1]), pk2s(a[2], a[3]), pk2s(b[0], b[1]), pk2s(b[2], b[3])});
            else { yw[2 * p * 64] = a; yw[(2 * p + 1) * 64] = b; }
        }
    };
    unsigned pf = 0;
    sfor<180>([&](auto S) {
        constexpr int s = S, c = s / 18, j = s % 18, k = j / 9, i = j % 9, t = s - 144;
        if constexpr (j == 16 && c < 8) { waitv_bar<(c < 7 ? 7 : 0)>(); PINH(c + 1); STAMPR(3 + c); }
        if constexpr (t == 4) { LDS_BAR(); STAMPR(11); }
        if constexpr (s < 142) fr[(s + 2) % 6] = wfrag(s + 2);
        else if constexpr (s < 144) { fr[(2 * s - 140) % 6] = wfrag(2 * s - 140); fr[(2 * s - 139) % 6] = wfrag(2 * s - 139); }
        else if constexpr (s + 4 < 180) fr[(s + 4) % 6] = wfrag(s + 4);
        if constexpr (t == 4) acc[1][8] = xi[8 * 64];
        if constexpr (t >= 4 && (t - 4) % 8 / 2 == 2) acc[1][ldt(t)] = xi[ldt(t) * 64];
        SBAR();
        if constexpr (s < 9) { mf16z(acc[0][i], fr[s % 6], hf[0][0]); mf16z(acc[1][i], fr[s % 6], hf[0][2]); }
        else if constexpr (s < 144) { mf16(acc[0][i], fr[s % 6], hf[c % 3][k]); mf16(acc[1][i], fr[s % 6], hf[c % 3][2 + k]); }
        else mf16(acc[0][ldt(t)], fr[s % 6], hf[2][lkk(t)]);
        SBAR();
        if constexpr (c < 7 && j % 2 == 0) item(std::integral_constant<int, c + 2>{}, std::integral_constant<int, j / 2>{});
        if constexpr (s == 126) gld1(xr, ws + O_X1T + (size_t)(tt * 36 + 9 * db + 2 * tg) * KB, xo);
        if constexpr (s == 128) asm volatile("global_load_dwordx2 %0, %1, %2" : "=&v"(x8) : "v"(xo8), "s"(ws + O_X1T + (size_t)(tt * 36 + 9 * db + 8) * KB) : "memory");
        if constexpr (s == 130) PF_CODE(pf, "_Z5k_qkvPc", 22 * KB);
        if constexpr (s >= 138 && s < 147) xw[(s - 138) * 64] = acc[1][s - 138];
        if constexpr (t == 8) send(IC(4));
        if constexpr (t >= 14 && (t - 14) % 8 == 0) send(std::integral_constant<int, (t - 14) / 8>{});
        SBAR();
    });
    asm volatile("s_nop 15" : "+v"(xr), "+v"(x8) :: "memory"); SBAR();
    send(IC(3));
    STAMPR(12);
    const u32x4 x4 = swap_pair(__builtin_bit_cast(u32x4, xr));
    v[2] += unpk(x8[0], x8[1]);
    waitvl_bar<0>();
    v[0] = yw[2 * tg * 64] + unpk(x4[0], x4[1]);
    v[1] = yw[(2 * tg + 1) * 64] + unpk(x4[2], x4[3]);
    STAMPR(13);
    const char *s0_ = ws + O_PART + (size_t)((rb * 8 + 2 * q + kh) * 4 + db) * 3 * 4608, *s1_ = s0_ + 4608, *s2_ = s0_ + 9216;
    unsigned po = l16 + tg * KB;
    u32x4 p[3];
    u32x2 p8[3] = {};
    #pragma nounroll
    for (int spin = 0; spin < SPIN_CAP; ++spin) {
#define LD3 "global_load_dwordx4 %0, %3, %4 sc1\n\tglobal_load_dwordx4 %1, %3, %5 sc1\n\tglobal_load_dwordx4 %2, %3, %6 sc1\n\t"
        if (tg == q)
            asm volatile(LD3 "global_load_dwordx2 %7, %10, %4 sc1\n\tglobal_load_dwordx2 %8, %10, %5 sc1\n\tglobal_load_dwordx2 %9, %10, %6 sc1\n\ts_waitcnt vmcnt(0)"
                         : "=&v"(p[0]), "=&v"(p[1]), "=&v"(p[2]), "+v"(po), "+s"(s0_), "+s"(s1_), "+s"(s2_), "=&v"(p8[0]), "=&v"(p8[1]), "=&v"(p8[2]) : "v"(4096 + lane * 8) : "memory");
        else
            asm volatile(LD3 "s_waitcnt vmcnt(0)" : "=&v"(p[0]), "=&v"(p[1]), "=&v"(p[2]) : "v"(po), "s"(s0_), "s"(s1_), "s"(s2_) : "memory");
        const unsigned m = max(max(max(umax4(p[0]), umax4(p[1])), max(umax4(p[2]), max(p8[0][0], p8[0][1]))), max(max(p8[1][0], p8[1][1]), max(p8[2][0], p8[2][1])));
        if (!__any(m == EMPTY)) break;
        __builtin_amdgcn_s_sleep(1);
    }
    STAMPR(14);
#pragma unroll
    for (int i = 0; i < 3; ++i) {
        v[0] += (f32x4){bflo(p[i][0]), bfhi(p[i][0]), bflo(p[i][1]), bfhi(p[i][1])};
        v[1] += (f32x4){bflo(p[i][2]), bfhi(p[i][2]), bflo(p[i][3]), bfhi(p[i][3])};
        v[2] += (f32x4){bflo(p8[i][0]), bfhi(p8[i][0]), bflo(p8[i][1]), bfhi(p8[i][1])};
    }
    unsigned o[6];
#pragma unroll
    for (int i = 0; i < 6; ++i) o[i] = pk2(v[i >> 1][2 * (i & 1)], v[i >> 1][2 * (i & 1) + 1]);
    const auto s0 = __builtin_amdgcn_permlane16_swap(o[0], o[2], false, false), s1 = __builtin_amdgcn_permlane16_swap(o[1], o[3], false, false);
    const u32x4 o4 = {s0[0], s1[0], s0[1], s1[1]};
    if (out) {
        char* ob = (char*)out + (size_t)(32 * tt + 16 * kh) * (D_ * 2) + db * 288;
        *(u32x4*)(ob + tg * 64 + (lane & 15) * (D_ * 2) + (g & 1) * 32 + (g >> 1) * 16) = o4;
        if (tg == q) *(u32x2*)(ob + 256 + (lane & 15) * (D_ * 2) + g * 8) = (u32x2){o[4], o[5]};
    } else {
        gst<0>(ws + O_XT + (size_t)(tt * 36 + 9 * db + 2 * tg) * KB, xo, o4);
        if (tg == q) *(u32x2*)(ws + O_XT + (size_t)(tt * 36 + 9 * db + 8) * KB + xo8) = (u32x2){o[4], o[5]};
        else o[4] = o[5] = 0;
        float a = 0.f, b = 0.f;
#pragma unroll
        for (int i = 0; i < 6; ++i) { const float l = bflo(o[i]), h = bfhi(o[i]); a += l + h; b += l * l + h * h; }
        const auto t = __builtin_amdgcn_permlane32_swap(__float_as_uint(a), __float_as_uint(b), false, false);
        const unsigned tu = __float_as_uint(__uint_as_float(t[0]) + __uint_as_float(t[1]));
        const auto r = __builtin_amdgcn_permlane16_swap(tu, tu, false, false);
        LDSP float* red = (LDSP float*)((lds_ptr)smem + 4 * SLOT);
        if (!(g & 1)) red[tg * 64 + (16 * kh + (lane & 15)) * 2 + (g >> 1)] = __uint_as_float(r[0]) + __uint_as_float(r[1]);
        LDS_BAR();
        if (w == 0) ((float*)(ws + O_STA))[(32 * tt + (lane >> 1)) * 8 + db * 2 + (lane & 1)] = (red[lane] + red[64 + lane]) + (red[128 + lane] + red[192 + lane]);
    }
    PF_DONE(pf);
    STAMPR(15); STAMP_OUT(4, 16);
}

extern "C" void kernel_launch(void* const* d_in, const int* in_sizes, int n_in, void* d_out, int out_size, void* d_ws,
                              size_t ws_size, hipStream_t stream) {
    const uint16_t* x = (const uint16_t*)d_in[0];
    const uint16_t* wqkv = (const uint16_t*)d_in[1];
    const uint16_t* wo = (const uint16_t*)d_in[2];
    const uint16_t* w1 = (const uint16_t*)d_in[4];
    const uint16_t* w3 = (const uint16_t*)d_in[5];
    const uint16_t* w2 = (const uint16_t*)d_in[6];
    char* ws = (char*)d_ws;
    uint16_t* out = (uint16_t*)d_out;
    (void)hipFuncSetAttribute((const void*)k_qkv, hipFuncAttributeMaxDynamicSharedMemorySize, QKV_LDS);
    (void)hipFuncSetAttribute((const void*)k_att<true>, hipFuncAttributeMaxDynamicSharedMemorySize, ATT_LDS);
    (void)hipFuncSetAttribute((const void*)k_att<false>, hipFuncAttributeMaxDynamicSharedMemorySize, ATT_LDS);
    (void)hipFuncSetAttribute((const void*)k_ff1, hipFuncAttributeMaxDynamicSharedMemorySize, FF_LDS);
    (void)hipFuncSetAttribute((const void*)k_ff2, hipFuncAttributeMaxDynamicSharedMemorySize, FF_LDS);
    k_prep<<<256, 192, 0, stream>>>(x, wqkv, wo, ws);
    for (int layer = 0; layer < 2; ++layer) {
        k_qkv<<<256, 256, QKV_LDS, stream>>>(ws);
        if (layer == 0) k_att<true><<<256, 512, ATT_LDS, stream>>>(ws, w1, w3, w2);
        else k_att<false><<<256, 512, ATT_LDS, stream>>>(ws, w1, w3, w2);
        k_ff1<<<256, 512, FF_LDS, stream>>>(ws);
        k_ff2<<<256, 512, FF_LDS, stream>>>(ws, layer ? out : nullptr);
    }
}
```
